```python
import torch

def _softmax(x, axis=-1):
    return torch.softmax(x, dim=axis)
def _gelu(x):
    return torch.nn.functional.gelu(x, approximate="tanh")
def _silu(x):
    return torch.nn.functional.silu(x)
def _sigmoid(x):
    return torch.sigmoid(x)
def _layernorm(x, axis=-1, eps=1e-5):
    m = x.mean(dim=axis, keepdim=True)
    v = x.var(dim=axis, keepdim=True, unbiased=False)
    return (x - m) / torch.sqrt(v + eps)
def _scatter_add(tgt, idx, src, axis=0):
    return tgt.clone().index_add_(axis, idx, src)
def _segment_sum(src, seg, n):
    out = torch.zeros((n,) + src.shape[1:], dtype=src.dtype)
    return out.index_add_(0, seg, src)
def _pad(x, pw):
    flat = tuple(v for pair in reversed(pw) for v in pair)
    return torch.nn.functional.pad(x, flat)

kernel_name = 'recurrent_scan__sliding_local_dilated__glu_gated_glu__prompt'

def setup_inputs():

    _w = (lambda a: (lambda t: t.to(torch.bfloat16) if t.is_floating_point() else t)(torch.as_tensor(a)))
    def _A_setup():
        B, S, D = 1, 2048, 512
        return {
            "x": _w((lambda *s: torch.randn(*s, dtype=torch.bfloat16) * 0.05)(B, S, D)),
            "wqkv": _w((lambda *s: torch.randn(*s, dtype=torch.bfloat16) * 0.05)(D, 3 * D)),
            "wo": _w((lambda *s: torch.randn(*s, dtype=torch.bfloat16) * 0.05)(D, D)),
        }
    _A_in = _A_setup()

    def _B_setup():
        B, S, D, F = 1, 2048, 512, 1792
        return {
            "x": _w((lambda *s: torch.randn(*s, dtype=torch.bfloat16) * 0.05)(B, S, D)),
            "w_gate": _w((lambda *s: torch.randn(*s, dtype=torch.bfloat16) * 0.05)(D, F)),
            "w_up": _w((lambda *s: torch.randn(*s, dtype=torch.bfloat16) * 0.05)(D, F)),
            "w_down": _w((lambda *s: torch.randn(*s, dtype=torch.bfloat16) * 0.05)(F, D)),
        }
    _B_in = _B_setup()

    return {
        "A_x": _A_in["x"],
        "A_wqkv": _A_in["wqkv"],
        "A_wo": _A_in["wo"],
        "B_x": _B_in["x"],
        "B_w_gate": _B_in["w_gate"],
        "B_w_up": _B_in["w_up"],
        "B_w_down": _B_in["w_down"],
    }

def reference(A_x, A_wqkv, A_wo, B_x, B_w_gate, B_w_up, B_w_down):
    def _A_ref(x, wqkv, wo):
        B, S, D = x.shape
        H = 32; Dh = D // H
        W, dil = 8, 2
        qkv = (lambda x, *d: x.permute(*d))((x @ wqkv).reshape(B, S, 3, H, Dh), 2, 0, 3, 1, 4)
        q, k, v = qkv[0], qkv[1], qkv[2]
        scale = Dh ** -0.5
        pos = torch.arange(S)
        off = torch.arange(W) * dil
        idx = pos[:, None] - off[None, :]
        mask = idx >= 0
        idx_c = torch.where(mask, idx, 0).reshape(-1)
        kg = k[:, :, idx_c, :].reshape(B, H, S, W, Dh)
        vg = v[:, :, idx_c, :].reshape(B, H, S, W, Dh)
        logits = torch.einsum("bhsd,bhswd->bhsw", q, kg) * scale
        logits = torch.where(mask[None, None, :, :], logits, -1e9)
        attn = _softmax(logits, axis=-1)
        o = torch.einsum("bhsw,bhswd->bhsd", attn, vg)
        return (lambda x, *d: x.permute(*d))(o, 0, 2, 1, 3).reshape(B, S, D) @ wo

    def _B_ref(x, w_gate, w_up, w_down):
        g = torch.einsum("bsd,df->bsf", x, w_gate)
        u = torch.einsum("bsd,df->bsf", x, w_up)
        h = _sigmoid(g) * u
        return torch.einsum("bsf,fd->bsd", h, w_down)

    x = A_x
    xn = _layernorm(x, axis=-1)
    s = _A_ref(xn, A_wqkv, A_wo)
    g = _silu(_B_ref(xn, B_w_gate, B_w_up, B_w_down))
    return x + s * g
```

```cpp
#include <hip/hip_runtime.h>
#include <hip/hip_bf16.h>
typedef unsigned short u16;
__device__ __forceinline__ float b2f(u16 b){ return __uint_as_float(((unsigned)b)<<16); }
__device__ __forceinline__ u16 f2b(float f){ unsigned u=__float_as_uint(f); u += 0x7fffu + ((u>>16)&1u); return (u16)(u>>16); }
__device__ __forceinline__ float rb(float f){ return b2f(f2b(f)); }
constexpr int S=2048, D=512, F=1792, H=32, DH=16;

__global__ void k_ln(const u16* x, u16* xn){
  int r=blockIdx.x, l=threadIdx.x;
  float v[8]; float s=0;
  for(int i=0;i<8;i++){ v[i]=b2f(x[r*D+l*8+i]); s+=v[i]; }
  for(int o=32;o;o>>=1) s+=__shfl_xor(s,o);
  float m=s/D; float q=0;
  for(int i=0;i<8;i++){ float d=v[i]-m; q+=d*d; }
  for(int o=32;o;o>>=1) q+=__shfl_xor(q,o);
  float var=rb(q/D); float mb=rb(m);
  float sd=rb(sqrtf(rb(var+1e-5f)));
  for(int i=0;i<8;i++) xn[r*D+l*8+i]=f2b(rb(v[i]-mb)/sd);
}
__global__ void k_gemm(const u16* A, const u16* B, u16* C, int M, int N, int K){
  __shared__ float As[16][17], Bs[16][17];
  int tx=threadIdx.x, ty=threadIdx.y; int row=blockIdx.y*16+ty, col=blockIdx.x*16+tx;
  float acc=0;
  for(int k0=0;k0<K;k0+=16){
    As[ty][tx]=b2f(A[row*K+k0+tx]); Bs[ty][tx]=b2f(B[(k0+ty)*N+col]);
    __syncthreads();
    for(int k=0;k<16;k++) acc+=As[ty][k]*Bs[k][tx];
    __syncthreads();
  }
  C[row*N+col]=f2b(acc);
}
__global__ void k_attn(const u16* qkv, u16* o){
  int idx=blockIdx.x*blockDim.x+threadIdx.x; int s=idx/H, h=idx%H;
  float q[DH]; for(int d=0;d<DH;d++) q[d]=b2f(qkv[s*3*D+h*DH+d]);
  float lg[8]; float mx=-1e30f;
  for(int w=0;w<8;w++){ int p=s-2*w; float a;
    if(p>=0){ a=0; for(int d=0;d<DH;d++) a+=q[d]*b2f(qkv[p*3*D+D+h*DH+d]); a=rb(rb(a)*0.25f);} else a=rb(-1e9f);
    lg[w]=a; mx=fmaxf(mx,a);}
  float sum=0; for(int w=0;w<8;w++){ lg[w]=__expf(lg[w]-mx); sum+=lg[w]; }
  float acc[DH]; for(int d=0;d<DH;d++) acc[d]=0;
  for(int w=0;w<8;w++){ int p=s-2*w; if(p<0) p=0; float a=rb(lg[w]/sum); for(int d=0;d<DH;d++) acc[d]+=a*b2f(qkv[p*3*D+2*D+h*DH+d]); }
  for(int d=0;d<DH;d++) o[s*D+h*DH+d]=f2b(acc[d]);
}
__global__ void k_glu(const u16* g, const u16* u, u16* h, int n){
  int i=blockIdx.x*blockDim.x+threadIdx.x; if(i<n){ float gg=b2f(g[i]); float sg=rb(1.f/(1.f+__expf(-gg))); h[i]=f2b(sg*b2f(u[i])); }
}
__global__ void k_fin(const u16* x, const u16* s, const u16* d, u16* out, int n){
  int i=blockIdx.x*blockDim.x+threadIdx.x; if(i<n){ float dd=b2f(d[i]); float g=rb(dd/(1.f+__expf(-dd))); out[i]=f2b(b2f(x[i])+rb(b2f(s[i])*g)); }
}
extern "C" void kernel_launch(void* const* d_in, const int* in_sizes, int n_in, void* d_out, int out_size, void* d_ws, size_t ws_size, hipStream_t stream){
  const u16* x=(const u16*)d_in[0]; const u16* wqkv=(const u16*)d_in[1]; const u16* wo=(const u16*)d_in[2];
  const u16* wg=(const u16*)d_in[4]; const u16* wu=(const u16*)d_in[5]; const u16* wd=(const u16*)d_in[6];
  u16* ws=(u16*)d_ws; u16* xn=ws; u16* qkv=xn+S*D; u16* o=qkv+S*3*D; u16* s=o+S*D; u16* g=s+S*D; u16* u=g+S*F; u16* h=u+S*F; u16* d=h+S*F;
  k_ln<<<S,64,0,stream>>>(x,xn);
  k_gemm<<<dim3(3*D/16,S/16),dim3(16,16),0,stream>>>(xn,wqkv,qkv,S,3*D,D);
  k_attn<<<S*H/256,256,0,stream>>>(qkv,o);
  k_gemm<<<dim3(D/16,S/16),dim3(16,16),0,stream>>>(o,wo,s,S,D,D);
  k_gemm<<<dim3(F/16,S/16),dim3(16,16),0,stream>>>(xn,wg,g,S,F,D);
  k_gemm<<<dim3(F/16,S/16),dim3(16,16),0,stream>>>(xn,wu,u,S,F,D);
  k_glu<<<S*F/256,256,0,stream>>>(g,u,h,S*F);
  k_gemm<<<dim3(D/16,S/16),dim3(16,16),0,stream>>>(h,wd,d,S,D,F);
  k_fin<<<S*D/256,256,0,stream>>>(x,s,d,(u16*)d_out,S*D);
}
```

```cpp
#include <hip/hip_runtime.h>

typedef unsigned short u16;
typedef unsigned u32;
typedef unsigned long long u64;
typedef short s16x4 __attribute__((ext_vector_type(4)));
typedef __bf16 bf16x8 __attribute__((ext_vector_type(8)));
typedef __bf16 bf16x2 __attribute__((ext_vector_type(2)));
typedef float f32x2 __attribute__((ext_vector_type(2)));
typedef float f32x4 __attribute__((ext_vector_type(4)));
typedef float f32x16 __attribute__((ext_vector_type(16)));
typedef u32 u32x2 __attribute__((ext_vector_type(2)));
typedef u32 u32x4 __attribute__((ext_vector_type(4)));
#define LAS __attribute__((address_space(3)))
#define GAS __attribute__((address_space(1)))
#define DI __device__ __forceinline__
#define SB() __builtin_amdgcn_sched_barrier(0)

#define ROTMODE 4

constexpr u32 WS_EPOCH = 0;
constexpr u32 WS_FLAGS = 4096;
constexpr u32 WS_STATS = 8192;
constexpr u32 WS_H = 65536;
constexpr u32 H_BYTES = 64u * 144u * 1024u;
constexpr u32 WS_STAMP = WS_H + H_BYTES;
constexpr u32 L_EXKV = 128 * 1024;
constexpr u32 L_EXCS = 144 * 1024;
constexpr u32 L_GRAN = 154 * 1024;
constexpr u32 L_RS = 157 * 1024;
constexpr u32 L_NM = L_RS + 576;
constexpr u32 L_RN = L_NM + 576;
constexpr u64 WTAB = 0x301D13003AULL;
constexpr u32 XTAB = 0x0A4427u;
constexpr u32 SPIN_CAP = 1u << 18;

DI f32x4 mfma16(u32x4 a, u32x4 b, f32x4 c) {
  return __builtin_amdgcn_mfma_f32_16x16x32_bf16(__builtin_bit_cast(bf16x8, a), __builtin_bit_cast(bf16x8, b), c, 0, 0, 0);
}
DI f32x4 mfma16k16(u32x2 a, u32x2 b, f32x4 c) {
  return __builtin_amdgcn_mfma_f32_16x16x16bf16_1k(__builtin_bit_cast(s16x4, a), __builtin_bit_cast(s16x4, b), c, 0, 0, 0);
}
DI f32x16 mfma32(u32x4 a, u32x4 b, f32x16 c) {
  return __builtin_amdgcn_mfma_f32_32x32x16_bf16(__builtin_bit_cast(bf16x8, a), __builtin_bit_cast(bf16x8, b), c, 0, 0, 0);
}
DI u32 pk(float a, float b) { f32x2 v = {a, b}; return __builtin_bit_cast(u32, __builtin_convertvector(v, bf16x2)); }
DI float rbf(float a) { return __uint_as_float(pk(a, a) << 16); }
DI float lo(u32 v) { return __uint_as_float(v << 16); }
DI float hi(u32 v) { return __uint_as_float(v & 0xffff0000u); }

DI void glds(u32 voff, const char* sbase, u32 dst) {
  asm volatile("s_mov_b32 m0, %2\n\ts_nop 0\n\tglobal_load_lds_dwordx4 %0, %1" ::"v"(voff), "s"(sbase), "s"(dst) : "memory");
}
DI void glds_sc1(u32 voff, const char* sbase, u32 dst) {
  asm volatile("s_mov_b32 m0, %2\n\ts_nop 0\n\tglobal_load_lds_dwordx4 %0, %1 sc1" ::"v"(voff), "s"(sbase), "s"(dst) : "memory");
}
DI void glds_if(u32 on, u32 voff, const char* sbase, u32 dst) {
  asm volatile("s_cmp_eq_u32 %3, 0\n\ts_cbranch_scc1 1f\n\ts_mov_b32 m0, %2\n\ts_nop 0\n\tglobal_load_lds_dwordx4 %0, %1\n1:" ::"v"(voff),
               "s"(sbase), "s"(dst), "s"(on)
               : "memory", "scc");
}
DI void glds64_if(u32 on, const char* vaddr, u32 dst) {
  asm volatile("s_cmp_eq_u32 %2, 0\n\ts_cbranch_scc1 1f\n\ts_mov_b32 m0, %1\n\ts_nop 0\n\tglobal_load_lds_dwordx4 %0, off\n1:" ::"v"(vaddr),
               "s"(dst), "s"(on)
               : "memory", "scc");
}
#define WAITV(n) asm volatile("s_waitcnt vmcnt(" #n ")" ::: "memory")
#define BAR() asm volatile("s_waitcnt lgkmcnt(0)\n\ts_barrier" ::: "memory")

DI u32x4 ld128(LAS char* p) { return *(LAS u32x4*)p; }
DI u32x4 ldtr(LAS char* p) {
  s16x4 a = __builtin_amdgcn_ds_read_tr16_b64_v4i16((LAS s16x4*)p);
  s16x4 b = __builtin_amdgcn_ds_read_tr16_b64_v4i16((LAS s16x4*)(p + 512));
  u32x2 x = __builtin_bit_cast(u32x2, a), y = __builtin_bit_cast(u32x2, b);
  return u32x4{x[0], x[1], y[0], y[1]};
}
template <int CTRL> DI float dpp(float v) {
  return __int_as_float(__builtin_amdgcn_update_dpp(0, __float_as_int(v), CTRL, 0xf, 0xf, true));
}
DI float xor16(float v) { auto r = __builtin_amdgcn_permlane16_swap(__float_as_uint(v), __float_as_uint(v), false, false);
  return __uint_as_float(r[0] ^ r[1] ^ __float_as_uint(v)); }
DI float add16(float v) { auto r = __builtin_amdgcn_permlane16_swap(__float_as_uint(v), __float_as_uint(v), false, false);
  return __uint_as_float(r[0]) + __uint_as_float(r[1]); }
DI float add32(float v) { auto r = __builtin_amdgcn_permlane32_swap(__float_as_uint(v), __float_as_uint(v), false, false);
  return __uint_as_float(r[0]) + __uint_as_float(r[1]); }
DI float max16(float v) { auto r = __builtin_amdgcn_permlane16_swap(__float_as_uint(v), __float_as_uint(v), false, false);
  return fmaxf(__uint_as_float(r[0]), __uint_as_float(r[1])); }
DI float max32(float v) { auto r = __builtin_amdgcn_permlane32_swap(__float_as_uint(v), __float_as_uint(v), false, false);
  return fmaxf(__uint_as_float(r[0]), __uint_as_float(r[1])); }
DI float sum32lanes(float v) {
  v += dpp<0xB1>(v);
  v += dpp<0x4E>(v);
  v += dpp<0x141>(v);
  v += dpp<0x140>(v);
  return add16(v);
}

#define STAMP(i)

struct Frag { u32x4 x[4]; u32x4 w[6]; u32x4 hx; };
struct Acc { f32x4 m[5][4]; f32x4 e[6]; };

struct Ctx {
  LAS char* lds;
  const char* img[5];
  const char* mixp;
  const char* xrow;
  const char* xhalo;
  const char* w2;
  const char* gran;
  u32 vA, vG, vX, vW2;
  u32 rW, rW4, rW5, rX, rH;
  u32 rot, c, w, lb;
};

template <int TYPE, int T, bool HALO>
DI void read_frags(const Ctx& k, Frag& f, u32 wa, u32 xa, int sub, int part) {
  LAS char* pw = k.lds + (wa + k.rW);
  LAS char* px = k.lds + (xa + (k.rX ^ (sub * 64)));
  switch (part) {
    case 0: f.x[0] = ld128(px); break;
    case 1: f.w[0] = ldtr(pw); break;
    case 2: f.x[1] = ld128(px + 2048); break;
    case 3: f.w[1] = ldtr(pw + 4096); break;
    case 4: if (TYPE == 0) f.x[2] = ld128(px + 4096); else f.x[2] = ld128(k.lds + (xa + ((k.rX ^ 4096) ^ (sub * 64)))); break;
    case 5: f.w[2] = ldtr(pw + 8192); break;
    case 6: if (TYPE == 0) f.x[3] = ld128(px + 6144); else f.x[3] = ld128(k.lds + (xa + ((k.rX ^ 4096) ^ (sub * 64))) + 2048); break;
    case 7: f.w[3] = ldtr(pw + 12288); break;
    case 8: if (TYPE == 0) f.w[4] = ldtr(pw + 16384); else f.w[4] = ldtr(k.lds + (wa + k.rW4)); break;
    case 9: if (TYPE == 1) f.w[5] = ldtr(k.lds + (wa + k.rW5));
            else if (HALO) f.hx = ld128(k.lds + (xa + (k.rH ^ (sub * 64)))); break;
  }
}

template <int TYPE> DI void main_mfma(Acc& a, const Frag& f, int n) {
  const int j = n >> 2, i = n & 3;
  if (TYPE == 0) {
    if (j == 2) a.m[j][i] = mfma16(f.x[i], f.w[j], a.m[j][i]);
    else a.m[j][i] = mfma16(f.w[j], f.x[i], a.m[j][i]);
  } else {
    if (j < 4) a.m[j][i] = mfma16(f.w[j], f.x[i], a.m[j][i]);
    else a.m[4][i] = mfma16(f.w[4 + (i >> 1)], f.x[i & 1], a.m[4][i]);
  }
}

struct Sites { const char* b[8]; u32 d[8]; u32 on45, on6, on7; u32 mixoff; };

template <int TYPE, int T> DI void make_sites(Ctx& k, Sites& q, int s, u32 wfree, u32 xfree) {
  if (s <= 10) {
    const u32 krow = ((s + 5) * 32 + k.rot * 64) & 511;
    const u32 oA = krow * (TYPE == 0 ? 3072u : 3584u), oG = krow * 3584u;
    q.b[0] = k.img[0] + oA; q.b[1] = k.img[1] + oA; q.b[2] = k.img[2] + oA; q.b[3] = k.img[3] + oG; q.b[4] = k.img[4] + oG;
    q.mixoff = oG;
    const u32 xk = ((((s + 5) >> 1) + k.rot) & 7) * 128;
    q.b[5] = k.xrow + xk; q.b[6] = k.xrow + xk + 32768; q.b[7] = k.xhalo + xk;
#pragma unroll
    for (int j = 0; j < 5; ++j) q.d[j] = k.lb + wfree + j * 4096 + k.c * 1024;
    q.d[5] = k.lb + xfree + T * 8192 + k.c * 1024; q.d[6] = q.d[5] + 4096; q.d[7] = k.lb + xfree + 16384 + k.c * 1024;
    q.on45 = 1; q.on6 = 1; q.on7 = ~k.c & 2;
  } else {
    const u32 e = (s - 11) >> 1;
    const u32 dst = k.lb + (e < 2 ? (T + 2 * e) * 24576 + k.c * 6144 : 98304 + T * 16384 + k.c * 4096);
#pragma unroll
    for (int j = 0; j < 6; ++j) { q.b[j] = k.w2 + (6 * e + j) * 8192; q.d[j] = dst + j * 1024; }
    q.mixoff = (6 * e + 4) * 8192;
    q.b[6] = q.b[0]; q.b[7] = q.b[0]; q.d[6] = dst; q.d[7] = dst;
    q.on45 = 2 - e; q.on6 = 0; q.on7 = 0;
  }
}
template <int TYPE, int T> DI void issue_site(const Ctx& k, const Sites& q, int n) {
  const int j = (T == 0) ? n : (n < 2 ? 5 + n : n - 2);
  if (j < 3) glds(TYPE == 0 ? k.vA : k.vG, q.b[j], q.d[j]);
  else if (j == 3) glds(k.vG, q.b[j], q.d[j]);
  else if (j == 4) { if (TYPE == 0) glds_if(q.on45, k.vG, q.b[4], q.d[4]); else glds64_if(q.on45, k.mixp + q.mixoff, q.d[4]); }
  else if (j == 5) glds_if(q.on45, k.vX, q.b[5], q.d[5]);
  else if (j == 6) glds_if(q.on6, k.vX, q.b[6], q.d[6]);
  else if (TYPE == 0 && T == 0) glds_if(q.on7, k.vX, q.b[7], q.d[7]);
}

template <int TYPE, int T, int PAR>
DI void kstep(Ctx& k, Acc& a, const Frag& cur, Frag& nxt, int s, u64& wtab, u32& xtab, u32& wprev, int lane) {
  constexpr bool ISSUE = (T == ((PAR + 1) & 1));
  const u32 wa = ((u32)wtab & 0xff) << 11;
  wtab = (wtab >> 8) | ((wtab & 0xff) << 32);
  if (PAR == 1) xtab = (xtab >> 8) | ((xtab & 0xff) << 16);
  const u32 xa = (xtab & 0xff) << 11;
  const u32 xfree = ((xtab >> 16) & 0xff) << 11;
  Sites q;
  if (ISSUE) {
    if (s == 11 + T) {
      k.vA = k.vW2; k.vG = k.vW2; k.vX = k.vW2; k.mixp = k.w2 + k.vW2;
      if (T == 0 && k.c < 3) glds_sc1(lane * 16 + k.c * 1024, k.gran, k.lb + L_GRAN + k.c * 1024);
    }
    make_sites<TYPE, T>(k, q, s, wprev, xfree);
  }
  wprev = wa;
  SB();
#pragma unroll
  for (int n = 0; n < 20; ++n) {
    main_mfma<TYPE>(a, cur, n);
    if (n < 10
    ) read_frags<TYPE, T, ISSUE>(k, nxt, wa, xa, (PAR + 1) & 1, n);
    if (ISSUE && n >= 3 && (n & 1) && (n - 3) / 2 < 8 - T) { SB(); issue_site<TYPE, T>(k, q, (n - 3) / 2); }
    SB();
  }
  if (!ISSUE) {
    if (TYPE == 0) {
      u32x4 hx = cur.hx;
      const bool one = (lane & 15) == 0;
#pragma unroll
      for (int i = 0; i < 4; ++i) hx[i] = one ? 0x3F803F80u : hx[i];
      a.e[0] = mfma16(cur.w[0], hx, a.e[0]); a.e[1] = mfma16(cur.w[1], hx, a.e[1]); a.e[2] = mfma16(hx, cur.w[2], a.e[2]);
      a.e[3] = mfma16(cur.w[3], hx, a.e[3]); a.e[4] = mfma16(cur.w[4], hx, a.e[4]);
    } else {
      const u32x4 ones = {0x3F803F80u, 0x3F803F80u, 0x3F803F80u, 0x3F803F80u};
#pragma unroll
      for (int j = 0; j < 6; ++j) a.e[j] = mfma16(cur.w[j], ones, a.e[j]);
    }
    SB();
  }
}

template <int TYPE, int T>
DI void kloop(Ctx& k, Acc& a, int lane, const char* xg, char* ws, u32 srow, u32& tlo, u32& thi, u64* stamp) {
  u32x2 ep; u32x4 r0, r1;
  asm volatile("global_load_dwordx2 %0, %1, %2 sc1" : "=v"(ep) : "v"(0u), "s"(ws) : "memory");
  if (T == 1) {
    const u32 o = (srow + (lane >> 5)) * 1024 + (lane & 31) * 16;
    asm volatile("global_load_dwordx4 %0, %2, %3\n\tglobal_load_dwordx4 %1, %2, %3 offset:512" : "=&v"(r0), "=&v"(r1) : "v"(o), "s"(xg) : "memory");
  }
  {
    Sites q;
    make_sites<TYPE, T>(k, q, T - 5, (u32)((WTAB >> (8 * T)) & 0xff) << 11, (XTAB & 0xff) << 11);
    if (T == 0) { issue_site<TYPE, T>(k, q, 5); issue_site<TYPE, T>(k, q, 6); issue_site<TYPE, T>(k, q, 7);
#pragma unroll
      for (int n = 0; n < 5; ++n) issue_site<TYPE, T>(k, q, n);
    } else {
#pragma unroll
      for (int n = 0; n < 7; ++n) issue_site<TYPE, T>(k, q, n);
    }
    make_sites<TYPE, T>(k, q, T - 3, (u32)((WTAB >> (8 * (T + 2))) & 0xff) << 11, ((XTAB >> 8) & 0xff) << 11);
#pragma unroll
    for (int n = 0; n < 8 - T; ++n) issue_site<TYPE, T>(k, q, n);
  }
  if (T == 0) asm volatile("s_waitcnt vmcnt(7)" : "+v"(ep)::"memory");
  else asm volatile("s_waitcnt vmcnt(12)" : "+v"(ep), "+v"(r0), "+v"(r1)::"memory");
  BAR();
  tlo = __builtin_amdgcn_readfirstlane(ep[0]) + 1; thi = __builtin_amdgcn_readfirstlane(ep[1]) + 1;
  Frag f0, f1;
#pragma unroll
  for (int n = 0; n < 10; ++n) read_frags<TYPE, T, T == 0>(k, f0, (u32)(WTAB & 0xff) << 11, (XTAB & 0xff) << 11, 0, n);
  if (T == 0) {
    Sites q;
    make_sites<TYPE, T>(k, q, -1, (u32)((WTAB >> 32) & 0xff) << 11, ((XTAB >> 16) & 0xff) << 11);
#pragma unroll
    for (int n = 0; n < 8; ++n) issue_site<TYPE, T>(k, q, n);
  } else {
    float v[16];
#pragma unroll
    for (int i = 0; i < 4; ++i) { v[2 * i] = lo(r0[i]); v[2 * i + 1] = hi(r0[i]); v[8 + 2 * i] = lo(r1[i]); v[9 + 2 * i] = hi(r1[i]); }
    float s = 0.f;
#pragma unroll
    for (int i = 0; i < 16; ++i) s += v[i];
    const float mean = sum32lanes(s) * (1.f / 512.f);
    float qq = 0.f;
#pragma unroll
    for (int i = 0; i < 16; ++i) { const float d = v[i] - mean; qq += d * d; }
    const float var = rbf(sum32lanes(qq) * (1.f / 512.f));
    const float sd = rbf(__builtin_sqrtf(rbf(var + 1e-5f)));
    const u32x4 g = {__float_as_uint(rbf(mean)), tlo, __float_as_uint(1.0f / sd), thi};
    if ((lane & 31) == 0) {
      const u32 o = WS_STATS + (srow + (lane >> 5)) * 16;
      asm volatile("global_store_dwordx4 %0, %1, %2 sc1\n\ts_nop 1" ::"v"(o), "v"(g), "s"(ws) : "memory");
    }
    WAITV(8);
  }
  STAMP(8);
  u64 wtab = (WTAB >> 8) | ((WTAB & 0xff) << 32);
  u32 xtab = XTAB, wprev = (u32)(WTAB & 0xff) << 11;
#pragma unroll 1
  for (int it = 0; it < 8; ++it) {
    SB(); BAR(); SB();
    kstep<TYPE, T, 0>(k, a, f0, f1, 2 * it, wtab, xtab, wprev, lane);
    if (T == 0) WAITV(6); else WAITV(11);
    SB(); BAR(); SB();
    kstep<TYPE, T, 1>(k, a, f1, f0, 2 * it + 1, wtab, xtab, wprev, lane);
    if (T == 1) asm volatile("s_waitcnt vmcnt(8)\n\ts_cmp_eq_u32 %0, 0\n\ts_cbranch_scc1 1f\n\ts_waitcnt vmcnt(6)\n1:" ::"s"(it) : "memory", "scc");
  }
  if (T == 1) {
#pragma unroll
    for (int j = 0; j < 4; ++j) glds(k.vW2, k.w2 + (12 + j) * 8192, k.lb + 98304 + 16384 + k.c * 4096 + j * 1024);
  }
}

DI float sigm_mul(float g2, float u) {
  return u * __builtin_amdgcn_rcpf(1.0f + __builtin_amdgcn_exp2f(g2));
}
DI u32x4 merge_tiles(u32x2 a, u32x2 b) {
  auto r0 = __builtin_amdgcn_permlane16_swap(a[0], b[0], false, false);
  auto r1 = __builtin_amdgcn_permlane16_swap(a[1], b[1], false, false);
  return u32x4{r0[0], r1[0], r0[1], r1[1]};
}
DI u32x2 glu_tile(f32x4 g, f32x4 u, f32x4 cg, f32x4 cu, f32x2 st) {
  const float NL = -1.4426950408889634f;
  const float ag = st[0] * NL, bg = st[1] * NL;
  float h[4];
#pragma unroll
  for (int r = 0; r < 4; ++r) h[r] = sigm_mul(g[r] * ag + bg * cg[r], u[r] * st[0] + st[1] * cu[r]);
  return u32x2{pk(h[0], h[1]), pk(h[2], h[3])};
}
DI u32x2 norm_tile(f32x4 v, f32x4 cs, f32x2 st) {
  return u32x2{pk(v[0] * st[0] + st[1] * cs[0], v[1] * st[0] + st[1] * cs[1]), pk(v[2] * st[0] + st[1] * cs[2], v[3] * st[0] + st[1] * cs[3])};
}

DI u32 slot_addr(u32 t, u32 c, int j) {
  return j < 6 ? (t + 2 * (j / 3)) * 24576 + c * 6144 + (j % 3) * 2048 : 98304 + t * 16384 + c * 4096 + (j - 6) * 2048;
}

__global__ __launch_bounds__(512, 2) void fused_block(const char* __restrict__ xg, const char* __restrict__ wqkv, const char* __restrict__ wo,
                                                      const char* __restrict__ wg, const char* __restrict__ wu, const char* __restrict__ wd,
                                                      char* __restrict__ out, char* __restrict__ ws) {
  __shared__ __attribute__((aligned(1024))) char smem[163840];
  u64 stamp[12];
  STAMP(0);
  int lane = threadIdx.x & 63;
  const u32 w = __builtin_amdgcn_readfirstlane(threadIdx.x >> 6), t = w >> 2, c = w & 3;
  const u32 b = blockIdx.x, xcd = b & 7, jb = b >> 3;
  const u32 panel = (xcd & 1) * 8 + (jb & 7), sl = jb >> 3, type = sl >> 1, idx = (xcd >> 1) * 2 + (sl & 1);
  const u32 slice = type * 8 + idx;
  const u32 rb = xcd * 4 + (jb & 3), cb = jb >> 2;
  u32 g = lane >> 4, n16 = lane & 15;
  const u32 kbeg = (u32)((0x7D6B105846332000ULL >> (8 * w)) & 0xff);
  const u32 knum = (u32)((0x1312101312131310ULL >> (8 * w)) & 0xff);

  Ctx k;
  k.lds = (LAS char*)smem;
  k.lb = (u32)(size_t)k.lds;
  k.c = c; k.w = w;
  k.rot = ((panel + sl) & 3) * 2;
  {
    const u32 rr = lane >> 3, pos = lane & 7;
    const u32 gcW = pos ^ ((((lane >> 4) & 1) | ((c & 1) << 1)) << 1);
    k.vA = (8 * c + rr) * 3072 + gcW * 16;
    k.vG = (8 * c + rr) * 3584 + gcW * 16;
    k.vX = (8 * c + rr) * 1024 + (pos ^ (4 * (c & 1) + (lane >> 4))) * 16;
    k.vW2 = rr * 1024 + (pos ^ (((lane >> 4) & 1) << 2)) * 16;
    const u32 q4 = n16 >> 2, p4 = lane & 3, f = (q4 >> 1) | ((g & 1) << 1);
    const u32 rowp = (8 * g + q4) * 128 + 8 * p4;
    k.rW = rowp + ((c ^ f) * 32);
    k.rW4 = 16384 + rowp + (((c >> 1) ^ f) * 32);
    k.rW5 = 16384 + rowp + (((2 + (c >> 1)) ^ f) * 32);
    const u32 xl = n16 * 128 + ((g ^ ((n16 >> 1) & 7)) * 16);
    k.rX = t * 8192 + xl + (type ? (c & 1) * 4096 : 0);
    k.rH = 16384 + xl;
    if (type == 0) {
      k.img[0] = wqkv + idx * 128; k.img[1] = k.img[0] + 1024; k.img[2] = k.img[0] + 2048;
      k.img[3] = wg + idx * 128; k.img[4] = wu + idx * 128;
      k.mixp = wg;
    } else {
      const u32 f0 = 1024 + 320 * idx, o0 = f0 + (idx & 1) * 64, om = (idx & 1) ? f0 : f0 + 256;
      k.img[0] = wg + o0; k.img[1] = wu + o0; k.img[2] = wg + o0 + 128; k.img[3] = wu + o0 + 128; k.img[4] = wg;
      k.mixp = (gcW < 4 ? wg : wu) + (om + (gcW & 3) * 16 + (8 * c + rr) * 3584);
    }
    k.xrow = xg + (panel * 128 + t * 64) * 1024;
    k.xhalo = xg + (panel ? panel * 128 - 16 : 0) * 1024;
    k.w2 = (kbeg < 32 ? wo + kbeg * 16384 : wd + (kbeg - 32) * 16384) + cb * 128;
    k.gran = ws + WS_STATS + ((int)panel * 128 - 16) * 16;
  }
  Acc a;
#pragma unroll
  for (int j = 0; j < 5; ++j)
#pragma unroll
    for (int i = 0; i < 4; ++i) a.m[j][i] = f32x4{0.f, 0.f, 0.f, 0.f};
#pragma unroll
  for (int j = 0; j < 6; ++j) a.e[j] = f32x4{0.f, 0.f, 0.f, 0.f};
  u32 tlo, thi;
  const u32 srow = panel * 128 + slice * 8 + c * 2;
  if (type == 0) { if (t == 0) kloop<0, 0>(k, a, lane, xg, ws, srow, tlo, thi, stamp); else kloop<0, 1>(k, a, lane, xg, ws, srow, tlo, thi, stamp); }
  else           { if (t == 0) kloop<1, 0>(k, a, lane, xg, ws, srow, tlo, thi, stamp); else kloop<1, 1>(k, a, lane, xg, ws, srow, tlo, thi, stamp); }
  STAMP(1);
  asm volatile("" : "+v"(lane));
  g = lane >> 4; n16 = lane & 15;
  LAS char* lds = k.lds;
  const __amdgpu_buffer_rsrc_t rsw = __builtin_amdgcn_make_buffer_rsrc((void*)ws, 0, WS_H + H_BYTES, 0x00020000);

  if (w < 3) {
    const u32 tok = w * 64 + lane;
    const bool need = tok < 144 && (tok >= 16 || (panel != 0 && type == 0));
    u32x4 gr = ld128(lds + L_GRAN + tok * 16);
    for (u32 spin = 0; spin < SPIN_CAP; ++spin) {
      const bool ok = !need || (gr[1] == tlo && gr[3] == thi);
      if (__all(ok)) break;
      __builtin_amdgcn_s_sleep(2);
      gr = __builtin_amdgcn_raw_buffer_load_b128(rsw, WS_STATS + (panel * 128 + (need ? tok : 16) - 16) * 16, 0, 16);
    }
    if (tok < 144) {
      const float rs = need ? __uint_as_float(gr[2]) : 0.f, nm = need ? -__uint_as_float(gr[0]) * rs : 0.f;
      *(LAS float*)(lds + L_RS + tok * 4) = rs;
      *(LAS float*)(lds + L_NM + tok * 4) = nm;
      *(LAS f32x2*)(lds + L_RN + tok * 8) = f32x2{rs, nm};
    }
  }
  {
    LAS char* cs = lds + L_EXCS + w * 384;
    if (n16 == 0) {
#pragma unroll
      for (int j = 0; j < 6; ++j) *(LAS f32x4*)(cs + j * 64 + g * 16) = a.e[j];
    }
    if (type == 0) {
      if (g == 0) *(LAS float*)(cs + 5 * 64 + n16 * 4) = a.e[2][0];
      *(LAS f32x4*)(lds + L_EXKV + w * 2048 + lane * 16) = t ? a.e[1] : a.m[1][3];
      *(LAS f32x4*)(lds + L_EXKV + w * 2048 + 1024 + lane * 16) = t ? a.e[2] : a.m[2][3];
    }
  }
  SB(); BAR(); SB();
  STAMP(2);
  u32x4 w2f[19][2];
  const u32 r2 = (lane >> 5) * 1024 + (n16 >> 2) * 128 + (((n16 >> 3) & 1) * 64) + ((lane >> 4) & 1) * 32 + (lane & 3) * 8;
  const u32 hbase = WS_H + (panel * 4 + t * 2) * (144 * 1024) + lane * 16;
  {
    f32x4 cs[6];
    LAS char* c0 = lds + L_EXCS + c * 384 + g * 16;
#pragma unroll
    for (int j = 0; j < 6; ++j) cs[j] = *(LAS f32x4*)(c0 + j * 64) + *(LAS f32x4*)(c0 + 4 * 384 + j * 64);
    if (type == 0) {
      const float csv = *(LAS float*)(lds + L_EXCS + c * 384 + 320 + n16 * 4) + *(LAS float*)(lds + L_EXCS + (c + 4) * 384 + 320 + n16 * 4);
      f32x4 hk = *(LAS f32x4*)(lds + L_EXKV + (w ^ 4) * 2048 + lane * 16), hv = *(LAS f32x4*)(lds + L_EXKV + (w ^ 4) * 2048 + 1024 + lane * 16);
      if (t == 0) { hk += a.e[1]; hv += a.e[2]; }
      f32x2 st[5];
#pragma unroll
      for (int i = 0; i < 5; ++i) st[i] = *(LAS f32x2*)(lds + L_RN + (t * 64 + i * 16 + n16) * 8);
      u32x2 hh[4];
#pragma unroll
      for (int i = 0; i < 4; ++i) hh[i] = glu_tile(a.m[3][i], a.m[4][i], cs[3], cs[4], st[i + 1]);
      const u32 ho = hbase + (32 + 4 * idx + c) * 1024;
      __builtin_amdgcn_raw_buffer_store_b128(merge_tiles(hh[0], hh[1]), rsw, ho, 0, 16);
      __builtin_amdgcn_raw_buffer_store_b128(merge_tiles(hh[2], hh[3]), rsw, ho + 144 * 1024, 0, 16);
      SB();
      WAITV(2);
#pragma unroll
      for (int j = 0; j < 8; ++j) { w2f[j][0] = ldtr(lds + slot_addr(t, c, j) + r2); w2f[j][1] = ldtr(lds + slot_addr(t, c, j) + (r2 ^ 64)); }
      u32x2 kf[5], vf[5], qf[4];
      kf[0] = norm_tile(hk, cs[1], st[0]);
#pragma unroll
      for (int i = 0; i < 4; ++i) { kf[i + 1] = norm_tile(a.m[1][i], cs[1], st[i + 1]); qf[i] = norm_tile(a.m[0][i], cs[0], st[i + 1]); }
#pragma unroll
      for (int i = 0; i < 5; ++i) {
        const f32x4 rs = *(LAS f32x4*)(lds + L_RS + (t * 64 + i * 16 + g * 4) * 4), nm = *(LAS f32x4*)(lds + L_NM + (t * 64 + i * 16 + g * 4) * 4);
        const f32x4 v = i ? a.m[2][i ? i - 1 : 0] : hv;
        vf[i] = u32x2{pk(v[0] * rs[0] + nm[0] * csv, v[1] * rs[1] + nm[1] * csv), pk(v[2] * rs[2] + nm[2] * csv, v[3] * rs[3] + nm[3] * csv)};
      }
      SB();
      asm volatile("s_waitcnt lgkmcnt(0)" ::: "memory");
#pragma unroll
      for (int j = 0; j < 16; ++j) glds(k.vW2, k.w2 + (16 + j) * 8192, k.lb + slot_addr(t, c, j >> 1) + (j & 1) * 1024);
      SB();
      const bool par = n16 & 1;
      const bool inB0 = 4 * g + par <= n16, inB1 = 4 * g + 2 + par <= n16;
      const bool first = (panel == 0 && t == 0);
      u32x2 of[4];
#pragma unroll
      for (int i = 0; i < 4; ++i) {
        const f32x4 z = {0.f, 0.f, 0.f, 0.f};
        const f32x4 sa = mfma16k16(kf[i], qf[i], z), sb = mfma16k16(kf[i + 1], qf[i], z);
        float a0 = par ? sa[1] : sa[0], a1 = par ? sa[3] : sa[2];
        if (i == 0 && first) { a0 = -4e9f; a1 = -4e9f; }
        const float l0 = inB0 ? (par ? sb[1] : sb[0]) : a0, l1 = inB1 ? (par ? sb[3] : sb[2]) : a1;
        const float mx = max32(max16(fmaxf(l0, l1)));
        const float SC = 0.25f * 1.4426950408889634f;
        const float p0 = __builtin_amdgcn_exp2f((l0 - mx) * SC), p1 = __builtin_amdgcn_exp2f((l1 - mx) * SC);
        const float inv = __builtin_amdgcn_rcpf(add32(add16(p0 + p1)));
        const u32 pp = pk(p0 * inv, p1 * inv);
        const u32 w0 = par ? (pp << 16) : (pp & 0xffffu), w1 = par ? (pp & 0xffff0000u) : (pp >> 16);
        const u32x4 pf = {inB0 ? 0u : w0, inB1 ? 0u : w1, inB0 ? w0 : 0u, inB1 ? w1 : 0u};
        const u32x4 vv = {vf[i][0], vf[i][1], vf[i + 1][0], vf[i + 1][1]};
        const f32x4 o = mfma16(vv, pf, z);
        of[i] = u32x2{pk(o[0], o[1]), pk(o[2], o[3])};
      }
      const u32 oo = hbase + (4 * idx + c) * 1024;
      __builtin_amdgcn_raw_buffer_store_b128(merge_tiles(of[0], of[1]), rsw, oo, 0, 16);
      __builtin_amdgcn_raw_buffer_store_b128(merge_tiles(of[2], of[3]), rsw, oo + 144 * 1024, 0, 16);
    } else {
      f32x2 st[4];
#pragma unroll
      for (int i = 0; i < 4; ++i) st[i] = *(LAS f32x2*)(lds + L_RN + (16 + t * 64 + ((i * 16) ^ ((c & 1) * 32)) + n16) * 8);
      const u32 s0 = 32 + 32 + 10 * idx;
      const u32 tA = (c & 1) * (144 * 1024), tB = 144 * 1024 - tA;
      const u32 h0 = hbase + (s0 + (idx & 1) * 2 + c) * 1024, hm = hbase + (s0 + ((idx & 1) ? 0 : 8) + (c >> 1)) * 1024;
      u32x2 hh[4];
#pragma unroll
      for (int i = 0; i < 4; ++i) hh[i] = glu_tile(a.m[0][i], a.m[1][i], cs[0], cs[1], st[i]);
      __builtin_amdgcn_raw_buffer_store_b128(merge_tiles(hh[0], hh[1]), rsw, h0 + tA, 0, 16);
      __builtin_amdgcn_raw_buffer_store_b128(merge_tiles(hh[2], hh[3]), rsw, h0 + tB, 0, 16);
      SB();
      WAITV(2);
#pragma unroll
      for (int j = 0; j < 8; ++j) { w2f[j][0] = ldtr(lds + slot_addr(t, c, j) + r2); w2f[j][1] = ldtr(lds + slot_addr(t, c, j) + (r2 ^ 64)); }
#pragma unroll
      for (int i = 0; i < 2; ++i) hh[i] = glu_tile(a.m[4][i], a.m[4][2 + i], cs[4], cs[5], st[i]);
      SB();
      asm volatile("s_waitcnt lgkmcnt(0)" ::: "memory");
#pragma unroll
      for (int j = 0; j < 16; ++j) glds(k.vW2, k.w2 + (16 + j) * 8192, k.lb + slot_addr(t, c, j >> 1) + (j & 1) * 1024);
      SB();
      __builtin_amdgcn_raw_buffer_store_b128(merge_tiles(hh[0], hh[1]), rsw, hm + tA, 0, 16);
#pragma unroll
      for (int i = 0; i < 4; ++i) hh[i] = glu_tile(a.m[2][i], a.m[3][i], cs[2], cs[3], st[i]);
      __builtin_amdgcn_raw_buffer_store_b128(merge_tiles(hh[0], hh[1]), rsw, h0 + 4096 + tA, 0, 16);
      __builtin_amdgcn_raw_buffer_store_b128(merge_tiles(hh[2], hh[3]), rsw, h0 + 4096 + tB, 0, 16);
    }
  }
  STAMP(3);
  SB(); WAITV(0); BAR(); SB();
  STAMP(4);
  const u64 tag = ((u64)thi << 32) | tlo;
  GAS u64* flags = (GAS u64*)(ws + WS_FLAGS);
  if (threadIdx.x == 0) __hip_atomic_store(flags + panel * 16 + slice, tag, __ATOMIC_RELAXED, __HIP_MEMORY_SCOPE_AGENT);
#pragma unroll
  for (int j = 0; j < 8; ++j) { w2f[8 + j][0] = ldtr(lds + slot_addr(t, c, j) + r2); w2f[8 + j][1] = ldtr(lds + slot_addr(t, c, j) + (r2 ^ 64)); }
  SB();
  asm volatile("s_waitcnt lgkmcnt(0)" ::: "memory");
#pragma unroll
  for (int j = 0; j < 6; ++j) glds_if(((1u << (knum - 16)) - 1) & (1u << (j >> 1)), k.vW2, k.w2 + (32 + j) * 8192, k.lb + slot_addr(t, c, j >> 1) + (j & 1) * 1024);
  SB();
  if (w == 0) {
    for (u32 spin = 0; spin < SPIN_CAP; ++spin) {
      const u64 v = __hip_atomic_load(flags + (rb >> 1) * 16 + n16, __ATOMIC_RELAXED, __HIP_MEMORY_SCOPE_AGENT);
      if (__all(v == tag)) break;
      __builtin_amdgcn_s_sleep(1);
    }
  }
  SB(); BAR(); SB();
  STAMP(5);
  const u32 orow = rb * 64 + 4 * w + g, ocol = cb * 128 + n16 * 8;
  u32x2 xres[2];
  {
    const u32 hsrc = WS_H + (rb * 2 * 144 + kbeg) * 1024 + lane * 16;
    constexpr int WIN = 10;
    u32x4 hf[WIN];
    auto hoff = [&](int n) -> u32 { const int tt = n / 19, st = n % 19; const u32 o = hsrc + (tt * 144 + st) * 1024;
                                    return st < 16 ? o : ((u32)st < knum ? o : 0x80000000u); };
#pragma unroll
    for (int n = 0; n < WIN; ++n) hf[n] = __builtin_amdgcn_raw_buffer_load_b128(rsw, hoff(n), 0, 16);
    SB();
    f32x16 acc[2];
#pragma unroll
    for (int n = 0; n < 38; ++n) {
      const int st = n % 19;
      if (st == 0) {
#pragma unroll
        for (int i = 0; i < 16; ++i) { acc[0][i] = 0.f; acc[1][i] = 0.f; }
      }
      if (n == 2) {
#pragma unroll
        for (int j = 0; j < 3; ++j) { w2f[16 + j][0] = ldtr(lds + slot_addr(t, c, j) + r2); w2f[16 + j][1] = ldtr(lds + slot_addr(t, c, j) + (r2 ^ 64)); }
      }
      acc[0] = mfma32(w2f[st][0], hf[n % WIN], acc[0]);
      acc[1] = mfma32(w2f[st][1], hf[n % WIN], acc[1]);
      SB();
      if (n + WIN < 38) hf[n % WIN] = __builtin_amdgcn_raw_buffer_load_b128(rsw, hoff(n + WIN), 0, 16);
      if (n + WIN == 38) {
        xres[0] = *(const GAS u32x2*)(xg + orow * 1024 + ocol);
        xres[1] = *(const GAS u32x2*)(xg + (orow + 32) * 1024 + ocol);
      }
      if (st == 18) {
        const int tt = n / 19;
#pragma unroll
        for (int u = 0; u < 8; ++u) {
          const u32 pos = (lane & 48) | ((lane ^ (2 * u + (lane >> 5))) & 15);
          const f32x4 v = {acc[u >> 2][4 * (u & 3)], acc[u >> 2][4 * (u & 3) + 1], acc[u >> 2][4 * (u & 3) + 2], acc[u >> 2][4 * (u & 3) + 3]};
          *(LAS f32x4*)(lds + slot_addr(t, c, (tt ? 0 : 4) + (u >> 1)) + (u & 1) * 1024 + pos * 16) = v;
        }
      }
      SB();
    }
  }
  if (b == 0 && w == 0) {
    for (u32 spin = 0; spin < SPIN_CAP; ++spin) {
      bool ok = true;
#pragma unroll
      for (int i = 0; i < 4; ++i) ok &= __hip_atomic_load(flags + i * 64 + lane, __ATOMIC_RELAXED, __HIP_MEMORY_SCOPE_AGENT) == tag;
      if (__all(ok)) break;
      __builtin_amdgcn_s_sleep(1);
    }
    if (lane == 0) __hip_atomic_store((GAS u64*)(ws + WS_EPOCH), tag, __ATOMIC_RELAXED, __HIP_MEMORY_SCOPE_AGENT);
  }
  STAMP(6);
  SB(); BAR(); SB();
  asm volatile("" : "+v"(lane));
  g = lane >> 4; n16 = lane & 15;
  {
    const u32 u = n16 >> 1, hh = n16 & 1, tok = 4 * w + g;
    const u32 pos = (hh * 32) | (tok & 16) | ((tok ^ n16) & 15);
#pragma unroll
    for (int tt = 0; tt < 2; ++tt) {
      f32x4 p[8];
#pragma unroll
      for (int ww = 0; ww < 8; ++ww) {
        const int j0 = tt ? 0 : 4;
        const u32 j = j0 + (u >> 1);
        const u32 sa = j < 6 ? ((ww >> 2) + 2 * (j / 3)) * 24576 + (ww & 3) * 6144 + (j % 3) * 2048 : 98304 + (ww >> 2) * 16384 + (ww & 3) * 4096 + (j - 6) * 2048;
        p[ww] = *(LAS f32x4*)(lds + sa + (u & 1) * 1024 + pos * 16);
      }
      const f32x4 s = p[0] + p[5], d = ((p[1] + p[2]) + (p[3] + p[4])) + (p[6] + p[7]);
      float r[4];
#pragma unroll
      for (int i = 0; i < 4; ++i) {
        const float dd = rbf(d[i]);
        const float gl = rbf(dd * __builtin_amdgcn_rcpf(1.0f + __builtin_amdgcn_exp2f(dd * -1.4426950408889634f)));
        const float xr = (i & 1) ? hi(xres[tt][i >> 1]) : lo(xres[tt][i >> 1]);
        r[i] = xr + rbf(rbf(s[i]) * gl);
      }
      *(GAS u32x2*)(out + (orow + 32 * tt) * 1024 + ocol) = u32x2{pk(r[0], r[1]), pk(r[2], r[3])};
    }
  }
}

extern "C" void kernel_launch(void* const* d_in, const int* in_sizes, int n_in, void* d_out, int out_size, void* d_ws, size_t ws_size,
                              hipStream_t stream) {
  fused_block<<<256, 512, 0, stream>>>((const char*)d_in[0], (const char*)d_in[1], (const char*)d_in[2], (const char*)d_in[4],
                                       (const char*)d_in[5], (const char*)d_in[6], (char*)d_out, (char*)d_ws);
}
```

```cpp
#define DIAG
#define SCI 0
#include <hip/hip_runtime.h>

typedef unsigned short u16;
typedef unsigned u32;
typedef unsigned long long u64;
typedef short s16x4 __attribute__((ext_vector_type(4)));
typedef __bf16 bf16x8 __attribute__((ext_vector_type(8)));
typedef __bf16 bf16x2 __attribute__((ext_vector_type(2)));
typedef float f32x2 __attribute__((ext_vector_type(2)));
typedef float f32x4 __attribute__((ext_vector_type(4)));
typedef float f32x16 __attribute__((ext_vector_type(16)));
typedef u32 u32x2 __attribute__((ext_vector_type(2)));
typedef u32 u32x4 __attribute__((ext_vector_type(4)));
#define LAS __attribute__((address_space(3)))
#define GAS __attribute__((address_space(1)))
#define DI __device__ __forceinline__
#define SB() __builtin_amdgcn_sched_barrier(0)

#define ROTMODE 4

constexpr u32 WS_EPOCH = 0;
constexpr u32 WS_FLAGS = 4096;
constexpr u32 WS_STATS = 8192;
constexpr u32 WS_H = 65536;
constexpr u32 H_BYTES = 64u * 144u * 1024u;
constexpr u32 WS_STAMP = WS_H + H_BYTES;
constexpr u32 L_EXKV = 128 * 1024;
constexpr u32 L_EXCS = 144 * 1024;
constexpr u32 L_GRAN = 154 * 1024;
constexpr u32 L_RS = 157 * 1024;
constexpr u32 L_NM = L_RS + 576;
constexpr u32 L_RN = L_NM + 576;
constexpr u64 WTAB = 0x301D13003AULL;
constexpr u32 XTAB = 0x0A4427u;
constexpr u32 SPIN_CAP = 1u << 18;

DI f32x4 mfma16(u32x4 a, u32x4 b, f32x4 c) {
  return __builtin_amdgcn_mfma_f32_16x16x32_bf16(__builtin_bit_cast(bf16x8, a), __builtin_bit_cast(bf16x8, b), c, 0, 0, 0);
}
DI f32x4 mfma16k16(u32x2 a, u32x2 b, f32x4 c) {
  return __builtin_amdgcn_mfma_f32_16x16x16bf16_1k(__builtin_bit_cast(s16x4, a), __builtin_bit_cast(s16x4, b), c, 0, 0, 0);
}
DI f32x16 mfma32(u32x4 a, u32x4 b, f32x16 c) {
  return __builtin_amdgcn_mfma_f32_32x32x16_bf16(__builtin_bit_cast(bf16x8, a), __builtin_bit_cast(bf16x8, b), c, 0, 0, 0);
}
DI u32 pk(float a, float b) { f32x2 v = {a, b}; return __builtin_bit_cast(u32, __builtin_convertvector(v, bf16x2)); }
DI float rbf(float a) { return __uint_as_float(pk(a, a) << 16); }
DI float lo(u32 v) { return __uint_as_float(v << 16); }
DI float hi(u32 v) { return __uint_as_float(v & 0xffff0000u); }

DI void glds(u32 voff, const char* sbase, u32 dst) {
  asm volatile("s_mov_b32 m0, %2\n\ts_nop 0\n\tglobal_load_lds_dwordx4 %0, %1" ::"v"(voff), "s"(sbase), "s"(dst) : "memory");
}
DI void glds_sc1(u32 voff, const char* sbase, u32 dst) {
  asm volatile("s_mov_b32 m0, %2\n\ts_nop 0\n\tglobal_load_lds_dwordx4 %0, %1 sc1" ::"v"(voff), "s"(sbase), "s"(dst) : "memory");
}
DI void glds_if(u32 on, u32 voff, const char* sbase, u32 dst) {
  asm volatile("s_cmp_eq_u32 %3, 0\n\ts_cbranch_scc1 1f\n\ts_mov_b32 m0, %2\n\ts_nop 0\n\tglobal_load_lds_dwordx4 %0, %1\n1:" ::"v"(voff),
               "s"(sbase), "s"(dst), "s"(on)
               : "memory", "scc");
}
DI void glds64_if(u32 on, const char* vaddr, u32 dst) {
  asm volatile("s_cmp_eq_u32 %2, 0\n\ts_cbranch_scc1 1f\n\ts_mov_b32 m0, %1\n\ts_nop 0\n\tglobal_load_lds_dwordx4 %0, off\n1:" ::"v"(vaddr),
               "s"(dst), "s"(on)
               : "memory", "scc");
}
#define WAITV(n) asm volatile("s_waitcnt vmcnt(" #n ")" ::: "memory")
#define BAR() asm volatile("s_waitcnt lgkmcnt(0)\n\ts_barrier" ::: "memory")

DI u32x4 ld128(LAS char* p) { return *(LAS u32x4*)p; }
DI u32x4 ldtr(LAS char* p) {
  s16x4 a = __builtin_amdgcn_ds_read_tr16_b64_v4i16((LAS s16x4*)p);
  s16x4 b = __builtin_amdgcn_ds_read_tr16_b64_v4i16((LAS s16x4*)(p + 512));
  u32x2 x = __builtin_bit_cast(u32x2, a), y = __builtin_bit_cast(u32x2, b);
  return u32x4{x[0], x[1], y[0], y[1]};
}
template <int CTRL> DI float dpp(float v) {
  return __int_as_float(__builtin_amdgcn_update_dpp(0, __float_as_int(v), CTRL, 0xf, 0xf, true));
}
DI float xor16(float v) { auto r = __builtin_amdgcn_permlane16_swap(__float_as_uint(v), __float_as_uint(v), false, false);
  return __uint_as_float(r[0] ^ r[1] ^ __float_as_uint(v)); }
DI float add16(float v) { auto r = __builtin_amdgcn_permlane16_swap(__float_as_uint(v), __float_as_uint(v), false, false);
  return __uint_as_float(r[0]) + __uint_as_float(r[1]); }
DI float add32(float v) { auto r = __builtin_amdgcn_permlane32_swap(__float_as_uint(v), __float_as_uint(v), false, false);
  return __uint_as_float(r[0]) + __uint_as_float(r[1]); }
DI float max16(float v) { auto r = __builtin_amdgcn_permlane16_swap(__float_as_uint(v), __float_as_uint(v), false, false);
  return fmaxf(__uint_as_float(r[0]), __uint_as_float(r[1])); }
DI float max32(float v) { auto r = __builtin_amdgcn_permlane32_swap(__float_as_uint(v), __float_as_uint(v), false, false);
  return fmaxf(__uint_as_float(r[0]), __uint_as_float(r[1])); }
DI float sum32lanes(float v) {
  v += dpp<0xB1>(v);
  v += dpp<0x4E>(v);
  v += dpp<0x141>(v);
  v += dpp<0x140>(v);
  return add16(v);
}

#define STAMP(i) do { SB(); u64 t_; asm volatile("s_memrealtime %0\n\ts_waitcnt lgkmcnt(0)" : "=s"(t_)::"memory"); stamp[i] = t_; SB(); } while (0)

struct Frag { u32x4 x[4]; u32x4 w[6]; u32x4 hx; };
struct Acc { f32x4 m[5][4]; f32x4 e[6]; };

struct Ctx {
  LAS char* lds;
  const char* img[5];
  const char* mixp;
  const char* xrow;
  const char* xhalo;
  const char* w2;
  const char* gran;
  u32 vA, vG, vX, vW2;
  u32 rW, rW4, rW5, rX, rH;
  u32 rot, c, w, lb;
};

template <int TYPE, int T, bool HALO>
DI void read_frags(const Ctx& k, Frag& f, u32 wa, u32 xa, int sub, int part) {
  LAS char* pw = k.lds + (wa + k.rW);
  LAS char* px = k.lds + (xa + (k.rX ^ (sub * 64)));
  switch (part) {
    case 0: f.x[0] = ld128(px); break;
    case 1: f.w[0] = ldtr(pw); break;
    case 2: f.x[1] = ld128(px + 2048); break;
    case 3: f.w[1] = ldtr(pw + 4096); break;
    case 4: if (TYPE == 0) f.x[2] = ld128(px + 4096); else f.x[2] = ld128(k.lds + (xa + ((k.rX ^ 4096) ^ (sub * 64)))); break;
    case 5: f.w[2] = ldtr(pw + 8192); break;
    case 6: if (TYPE == 0) f.x[3] = ld128(px + 6144); else f.x[3] = ld128(k.lds + (xa + ((k.rX ^ 4096) ^ (sub * 64))) + 2048); break;
    case 7: f.w[3] = ldtr(pw + 12288); break;
    case 8: if (TYPE == 0) f.w[4] = ldtr(pw + 16384); else f.w[4] = ldtr(k.lds + (wa + k.rW4)); break;
    case 9: if (TYPE == 1) f.w[5] = ldtr(k.lds + (wa + k.rW5));
            else if (HALO) f.hx = ld128(k.lds + (xa + (k.rH ^ (sub * 64)))); break;
  }
}

template <int TYPE> DI void main_mfma(Acc& a, const Frag& f, int n) {
  const int j = n >> 2, i = n & 3;
  if (TYPE == 0) {
    if (j == 2) a.m[j][i] = mfma16(f.x[i], f.w[j], a.m[j][i]);
    else a.m[j][i] = mfma16(f.w[j], f.x[i], a.m[j][i]);
  } else {
    if (j < 4) a.m[j][i] = mfma16(f.w[j], f.x[i], a.m[j][i]);
    else a.m[4][i] = mfma16(f.w[4 + (i >> 1)], f.x[i & 1], a.m[4][i]);
  }
}

struct Sites { const char* b[8]; u32 d[8]; u32 on45, on6, on7; u32 mixoff; };

template <int TYPE, int T> DI void make_sites(Ctx& k, Sites& q, int s, u32 wfree, u32 xfree) {
  if (s <= 10) {
    const u32 krow = ((s + 5) * 32 + k.rot * 64) & 511;
    const u32 oA = krow * (TYPE == 0 ? 3072u : 3584u), oG = krow * 3584u;
    q.b[0] = k.img[0] + oA; q.b[1] = k.img[1] + oA; q.b[2] = k.img[2] + oA; q.b[3] = k.img[3] + oG; q.b[4] = k.img[4] + oG;
    q.mixoff = oG;
    const u32 xk = ((((s + 5) >> 1) + k.rot) & 7) * 128;
    q.b[5] = k.xrow + xk; q.b[6] = k.xrow + xk + 32768; q.b[7] = k.xhalo + xk;
#pragma unroll
    for (int j = 0; j < 5; ++j) q.d[j] = k.lb + wfree + j * 4096 + k.c * 1024;
    q.d[5] = k.lb + xfree + T * 8192 + k.c * 1024; q.d[6] = q.d[5] + 4096; q.d[7] = k.lb + xfree + 16384 + k.c * 1024;
    q.on45 = 1; q.on6 = 1; q.on7 = ~k.c & 2;
  } else {
    const u32 e = (s - 11) >> 1;
    const u32 dst = k.lb + (e < 2 ? (T + 2 * e) * 24576 + k.c * 6144 : 98304 + T * 16384 + k.c * 4096);
#pragma unroll
    for (int j = 0; j < 6; ++j) { q.b[j] = k.w2 + (6 * e + j) * 8192; q.d[j] = dst + j * 1024; }
    q.mixoff = (6 * e + 4) * 8192;
    q.b[6] = q.b[0]; q.b[7] = q.b[0]; q.d[6] = dst; q.d[7] = dst;
    q.on45 = 2 - e; q.on6 = 0; q.on7 = 0;
  }
}
template <int TYPE, int T> DI void issue_site(const Ctx& k, const Sites& q, int n) {
  const int j = (T == 0) ? n : (n < 2 ? 5 + n : n - 2);
  if (j < 3) glds(TYPE == 0 ? k.vA : k.vG, q.b[j], q.d[j]);
  else if (j == 3) glds(k.vG, q.b[j], q.d[j]);
  else if (j == 4) { if (TYPE == 0) glds_if(q.on45, k.vG, q.b[4], q.d[4]); else glds64_if(q.on45, k.mixp + q.mixoff, q.d[4]); }
  else if (j == 5) glds_if(q.on45, k.vX, q.b[5], q.d[5]);
  else if (j == 6) glds_if(q.on6, k.vX, q.b[6], q.d[6]);
  else if (TYPE == 0 && T == 0) glds_if(q.on7, k.vX, q.b[7], q.d[7]);
}

template <int TYPE, int T, int PAR>
DI void kstep(Ctx& k, Acc& a, const Frag& cur, Frag& nxt, int s, u64& wtab, u32& xtab, u32& wprev, int lane) {
  constexpr bool ISSUE = (T == ((PAR + 1) & 1));
  const u32 wa = ((u32)wtab & 0xff) << 11;
  wtab = (wtab >> 8) | ((wtab & 0xff) << 32);
  if (PAR == 1) xtab = (xtab >> 8) | ((xtab & 0xff) << 16);
  const u32 xa = (xtab & 0xff) << 11;
  const u32 xfree = ((xtab >> 16) & 0xff) << 11;
  Sites q;
  if (ISSUE) {
    if (s == 11 + T) {
      k.vA = k.vW2; k.vG = k.vW2; k.vX = k.vW2; k.mixp = k.w2 + k.vW2;
      if (T == 0 && k.c < 3) glds_sc1(lane * 16 + k.c * 1024, k.gran, k.lb + L_GRAN + k.c * 1024);
    }
    make_sites<TYPE, T>(k, q, s, wprev, xfree);
  }
  wprev = wa;
  SB();
#pragma unroll
  for (int n = 0; n < 20; ++n) {
    main_mfma<TYPE>(a, cur, n);
    if (n < 10
    ) read_frags<TYPE, T, ISSUE>(k, nxt, wa, xa, (PAR + 1) & 1, n);
    if (ISSUE && n >= 3 && (n & 1) && (n - 3) / 2 < 8 - T) { SB(); issue_site<TYPE, T>(k, q, (n - 3) / 2); }
    SB();
  }
  if (!ISSUE) {
    if (TYPE == 0) {
      u32x4 hx = cur.hx;
      const bool one = (lane & 15) == 0;
#pragma unroll
      for (int i = 0; i < 4; ++i) hx[i] = one ? 0x3F803F80u : hx[i];
      a.e[0] = mfma16(cur.w[0], hx, a.e[0]); a.e[1] = mfma16(cur.w[1], hx, a.e[1]); a.e[2] = mfma16(hx, cur.w[2], a.e[2]);
      a.e[3] = mfma16(cur.w[3], hx, a.e[3]); a.e[4] = mfma16(cur.w[4], hx, a.e[4]);
    } else {
      const u32x4 ones = {0x3F803F80u, 0x3F803F80u, 0x3F803F80u, 0x3F803F80u};
#pragma unroll
      for (int j = 0; j < 6; ++j) a.e[j] = mfma16(cur.w[j], ones, a.e[j]);
    }
    SB();
  }
}

template <int TYPE, int T>
DI void kloop(Ctx& k, Acc& a, int lane, const char* xg, char* ws, u32 srow, u32& tlo, u32& thi, u64* stamp) {
  u32x2 ep; u32x4 r0, r1;
  asm volatile("global_load_dwordx2 %0, %1, %2 sc1" : "=v"(ep) : "v"(0u), "s"(ws) : "memory");
  if (T == 1) {
    const u32 o = (srow + (lane >> 5)) * 1024 + (lane & 31) * 16;
    asm volatile("global_load_dwordx4 %0, %2, %3\n\tglobal_load_dwordx4 %1, %2, %3 offset:512" : "=&v"(r0), "=&v"(r1) : "v"(o), "s"(xg) : "memory");
  }
  {
    Sites q;
    make_sites<TYPE, T>(k, q, T - 5, (u32)((WTAB >> (8 * T)) & 0xff) << 11, (XTAB & 0xff) << 11);
    if (T == 0) { issue_site<TYPE, T>(k, q, 5); issue_site<TYPE, T>(k, q, 6); issue_site<TYPE, T>(k, q, 7);
#pragma unroll
      for (int n = 0; n < 5; ++n) issue_site<TYPE, T>(k, q, n);
    } else {
#pragma unroll
      for (int n = 0; n < 7; ++n) issue_site<TYPE, T>(k, q, n);
    }
    make_sites<TYPE, T>(k, q, T - 3, (u32)((WTAB >> (8 * (T + 2))) & 0xff) << 11, ((XTAB >> 8) & 0xff) << 11);
#pragma unroll
    for (int n = 0; n < 8 - T; ++n) issue_site<TYPE, T>(k, q, n);
  }
  if (T == 0) asm volatile("s_waitcnt vmcnt(7)" : "+v"(ep)::"memory");
  else asm volatile("s_waitcnt vmcnt(12)" : "+v"(ep), "+v"(r0), "+v"(r1)::"memory");
  BAR();
  tlo = __builtin_amdgcn_readfirstlane(ep[0]) + 1; thi = __builtin_amdgcn_readfirstlane(ep[1]) + 1;
  Frag f0, f1;
#pragma unroll
  for (int n = 0; n < 10; ++n) read_frags<TYPE, T, T == 0>(k, f0, (u32)(WTAB & 0xff) << 11, (XTAB & 0xff) << 11, 0, n);
  if (T == 0) {
    Sites q;
    make_sites<TYPE, T>(k, q, -1, (u32)((WTAB >> 32) & 0xff) << 11, ((XTAB >> 16) & 0xff) << 11);
#pragma unroll
    for (int n = 0; n < 8; ++n) issue_site<TYPE, T>(k, q, n);
  } else {
    float v[16];
#pragma unroll
    for (int i = 0; i < 4; ++i) { v[2 * i] = lo(r0[i]); v[2 * i + 1] = hi(r0[i]); v[8 + 2 * i] = lo(r1[i]); v[9 + 2 * i] = hi(r1[i]); }
    float s = 0.f;
#pragma unroll
    for (int i = 0; i < 16; ++i) s += v[i];
    const float mean = sum32lanes(s) * (1.f / 512.f);
    float qq = 0.f;
#pragma unroll
    for (int i = 0; i < 16; ++i) { const float d = v[i] - mean; qq += d * d; }
    const float var = rbf(sum32lanes(qq) * (1.f / 512.f));
    const float sd = rbf(__builtin_sqrtf(rbf(var + 1e-5f)));
    const u32x4 g = {__float_as_uint(rbf(mean)), tlo, __float_as_uint(1.0f / sd), thi};
    if ((lane & 31) == 0) {
      const u32 o = WS_STATS + (srow + (lane >> 5)) * 16;
      asm volatile("global_store_dwordx4 %0, %1, %2 sc1\n\ts_nop 1" ::"v"(o), "v"(g), "s"(ws) : "memory");
    }
    WAITV(8);
  }
  STAMP(8);
  stamp[9] = __builtin_amdgcn_s_memtime();
  u64 wtab = (WTAB >> 8) | ((WTAB & 0xff) << 32);
  u32 xtab = XTAB, wprev = (u32)(WTAB & 0xff) << 11;
#pragma unroll 1
  for (int it = 0; it < 8; ++it) {
    SB(); BAR(); SB();
    kstep<TYPE, T, 0>(k, a, f0, f1, 2 * it, wtab, xtab, wprev, lane);
    if (T == 0) WAITV(6); else WAITV(11);
    SB(); BAR(); SB();
    kstep<TYPE, T, 1>(k, a, f1, f0, 2 * it + 1, wtab, xtab, wprev, lane);
    if (T == 1) asm volatile("s_waitcnt vmcnt(8)\n\ts_cmp_eq_u32 %0, 0\n\ts_cbranch_scc1 1f\n\ts_waitcnt vmcnt(6)\n1:" ::"s"(it) : "memory", "scc");
  }
  stamp[9] = __builtin_amdgcn_s_memtime() - stamp[9];
  if (T == 1) {
#pragma unroll
    for (int j = 0; j < 4; ++j) glds(k.vW2, k.w2 + (12 + j) * 8192, k.lb + 98304 + 16384 + k.c * 4096 + j * 1024);
  }
}

DI float sigm_mul(float g2, float u) {
  return u * __builtin_amdgcn_rcpf(1.0f + __builtin_amdgcn_exp2f(g2));
}
DI u32x4 merge_tiles(u32x2 a, u32x2 b) {
  auto r0 = __builtin_amdgcn_permlane16_swap(a[0], b[0], false, false);
  auto r1 = __builtin_amdgcn_permlane16_swap(a[1], b[1], false, false);
  return u32x4{r0[0], r1[0], r0[1], r1[1]};
}
DI u32x2 glu_tile(f32x4 g, f32x4 u, f32x4 cg, f32x4 cu, f32x2 st) {
  const float NL = -1.4426950408889634f;
  const float ag = st[0] * NL, bg = st[1] * NL;
  float h[4];
#pragma unroll
  for (int r = 0; r < 4; ++r) h[r] = sigm_mul(g[r] * ag + bg * cg[r], u[r] * st[0] + st[1] * cu[r]);
  return u32x2{pk(h[0], h[1]), pk(h[2], h[3])};
}
DI u32x2 norm_tile(f32x4 v, f32x4 cs, f32x2 st) {
  return u32x2{pk(v[0] * st[0] + st[1] * cs[0], v[1] * st[0] + st[1] * cs[1]), pk(v[2] * st[0] + st[1] * cs[2], v[3] * st[0] + st[1] * cs[3])};
}

DI u32 slot_addr(u32 t, u32 c, int j) {
  return j < 6 ? (t + 2 * (j / 3)) * 24576 + c * 6144 + (j % 3) * 2048 : 98304 + t * 16384 + c * 4096 + (j - 6) * 2048;
}

__global__ __launch_bounds__(512, 2) void fused_block(const char* __restrict__ xg, const char* __restrict__ wqkv, const char* __restrict__ wo,
                                                      const char* __restrict__ wg, const char* __restrict__ wu, const char* __restrict__ wd,
                                                      char* __restrict__ out, char* __restrict__ ws) {
  __shared__ __attribute__((aligned(1024))) char smem[163840];
  u64 stamp[12];
  STAMP(0);
  int lane = threadIdx.x & 63;
  const u32 w = __builtin_amdgcn_readfirstlane(threadIdx.x >> 6), t = w >> 2, c = w & 3;
  const u32 b = blockIdx.x, xcd = b & 7, jb = b >> 3;
  const u32 panel = (xcd & 1) * 8 + (jb & 7), sl = jb >> 3, type = sl >> 1, idx = (xcd >> 1) * 2 + (sl & 1);
  const u32 slice = type * 8 + idx;
  const u32 rb = xcd * 4 + (jb & 3), cb = jb >> 2;
  u32 g = lane >> 4, n16 = lane & 15;
  const u32 kbeg = (u32)((0x7D6B105846332000ULL >> (8 * w)) & 0xff);
  const u32 knum = (u32)((0x1312101312131310ULL >> (8 * w)) & 0xff);

  Ctx k;
  k.lds = (LAS char*)smem;
  k.lb = (u32)(size_t)k.lds;
  k.c = c; k.w = w;
  k.rot = ((panel + sl) & 3) * 2;
  {
    const u32 rr = lane >> 3, pos = lane & 7;
    const u32 gcW = pos ^ ((((lane >> 4) & 1) | ((c & 1) << 1)) << 1);
    k.vA = (8 * c + rr) * 3072 + gcW * 16;
    k.vG = (8 * c + rr) * 3584 + gcW * 16;
    k.vX = (8 * c + rr) * 1024 + (pos ^ (4 * (c & 1) + (lane >> 4))) * 16;
    k.vW2 = rr * 1024 + (pos ^ (((lane >> 4) & 1) << 2)) * 16;
    const u32 q4 = n16 >> 2, p4 = lane & 3, f = (q4 >> 1) | ((g & 1) << 1);
    const u32 rowp = (8 * g + q4) * 128 + 8 * p4;
    k.rW = rowp + ((c ^ f) * 32);
    k.rW4 = 16384 + rowp + (((c >> 1) ^ f) * 32);
    k.rW5 = 16384 + rowp + (((2 + (c >> 1)) ^ f) * 32);
    const u32 xl = n16 * 128 + ((g ^ ((n16 >> 1) & 7)) * 16);
    k.rX = t * 8192 + xl + (type ? (c & 1) * 4096 : 0);
    k.rH = 16384 + xl;
    if (type == 0) {
      k.img[0] = wqkv + idx * 128; k.img[1] = k.img[0] + 1024; k.img[2] = k.img[0] + 2048;
      k.img[3] = wg + idx * 128; k.img[4] = wu + idx * 128;
      k.mixp = wg;
    } else {
      const u32 f0 = 1024 + 320 * idx, o0 = f0 + (idx & 1) * 64, om = (idx & 1) ? f0 : f0 + 256;
      k.img[0] = wg + o0; k.img[1] = wu + o0; k.img[2] = wg + o0 + 128; k.img[3] = wu + o0 + 128; k.img[4] = wg;
      k.mixp = (gcW < 4 ? wg : wu) + (om + (gcW & 3) * 16 + (8 * c + rr) * 3584);
    }
    k.xrow = xg + (panel * 128 + t * 64) * 1024;
    k.xhalo = xg + (panel ? panel * 128 - 16 : 0) * 1024;
    k.w2 = (kbeg < 32 ? wo + kbeg * 16384 : wd + (kbeg - 32) * 16384) + cb * 128;
    k.gran = ws + WS_STATS + ((int)panel * 128 - 16) * 16;
  }
  Acc a;
#pragma unroll
  for (int j = 0; j < 5; ++j)
#pragma unroll
    for (int i = 0; i < 4; ++i) a.m[j][i] = f32x4{0.f, 0.f, 0.f, 0.f};
#pragma unroll
  for (int j = 0; j < 6; ++j) a.e[j] = f32x4{0.f, 0.f, 0.f, 0.f};
  u32 tlo, thi;
  const u32 srow = panel * 128 + slice * 8 + c * 2;
  if (type == 0) { if (t == 0) kloop<0, 0>(k, a, lane, xg, ws, srow, tlo, thi, stamp); else kloop<0, 1>(k, a, lane, xg, ws, srow, tlo, thi, stamp); }
  else           { if (t == 0) kloop<1, 0>(k, a, lane, xg, ws, srow, tlo, thi, stamp); else kloop<1, 1>(k, a, lane, xg, ws, srow, tlo, thi, stamp); }
  STAMP(1);
  asm volatile("" : "+v"(lane));
  g = lane >> 4; n16 = lane & 15;
  LAS char* lds = k.lds;
  const __amdgpu_buffer_rsrc_t rsw = __builtin_amdgcn_make_buffer_rsrc((void*)ws, 0, WS_H + H_BYTES, 0x00020000);

  if (w < 3) {
    const u32 tok = w * 64 + lane;
    const bool need = tok < 144 && (tok >= 16 || (panel != 0 && type == 0));
    u32x4 gr = ld128(lds + L_GRAN + tok * 16);
    for (u32 spin = 0; spin < SPIN_CAP; ++spin) {
      const bool ok = !need || (gr[1] == tlo && gr[3] == thi);
      if (__all(ok)) break;
      __builtin_amdgcn_s_sleep(2);
      gr = __builtin_amdgcn_raw_buffer_load_b128(rsw, WS_STATS + (panel * 128 + (need ? tok : 16) - 16) * 16, 0, 16);
    }
    if (tok < 144) {
      const float rs = need ? __uint_as_float(gr[2]) : 0.f, nm = need ? -__uint_as_float(gr[0]) * rs : 0.f;
      *(LAS float*)(lds + L_RS + tok * 4) = rs;
      *(LAS float*)(lds + L_NM + tok * 4) = nm;
      *(LAS f32x2*)(lds + L_RN + tok * 8) = f32x2{rs, nm};
    }
  }
  {
    LAS char* cs = lds + L_EXCS + w * 384;
    if (n16 == 0) {
#pragma unroll
      for (int j = 0; j < 6; ++j) *(LAS f32x4*)(cs + j * 64 + g * 16) = a.e[j];
    }
    if (type == 0) {
      if (g == 0) *(LAS float*)(cs + 5 * 64 + n16 * 4) = a.e[2][0];
      *(LAS f32x4*)(lds + L_EXKV + w * 2048 + lane * 16) = t ? a.e[1] : a.m[1][3];
      *(LAS f32x4*)(lds + L_EXKV + w * 2048 + 1024 + lane * 16) = t ? a.e[2] : a.m[2][3];
    }
  }
  SB(); BAR(); SB();
  STAMP(2);
  u32x4 w2f[19][2];
  const u32 r2 = (lane >> 5) * 1024 + (n16 >> 2) * 128 + (((n16 >> 3) & 1) * 64) + ((lane >> 4) & 1) * 32 + (lane & 3) * 8;
  const u32 hbase = WS_H + (panel * 4 + t * 2) * (144 * 1024) + lane * 16;
  {
    f32x4 cs[6];
    LAS char* c0 = lds + L_EXCS + c * 384 + g * 16;
#pragma unroll
    for (int j = 0; j < 6; ++j) cs[j] = *(LAS f32x4*)(c0 + j * 64) + *(LAS f32x4*)(c0 + 4 * 384 + j * 64);
    if (type == 0) {
      const float csv = *(LAS float*)(lds + L_EXCS + c * 384 + 320 + n16 * 4) + *(LAS float*)(lds + L_EXCS + (c + 4) * 384 + 320 + n16 * 4);
      f32x4 hk = *(LAS f32x4*)(lds + L_EXKV + (w ^ 4) * 2048 + lane * 16), hv = *(LAS f32x4*)(lds + L_EXKV + (w ^ 4) * 2048 + 1024 + lane * 16);
      if (t == 0) { hk += a.e[1]; hv += a.e[2]; }
      f32x2 st[5];
#pragma unroll
      for (int i = 0; i < 5; ++i) st[i] = *(LAS f32x2*)(lds + L_RN + (t * 64 + i * 16 + n16) * 8);
      u32x2 hh[4];
#pragma unroll
      for (int i = 0; i < 4; ++i) hh[i] = glu_tile(a.m[3][i], a.m[4][i], cs[3], cs[4], st[i + 1]);
      const u32 ho = hbase + (32 + 4 * idx + c) * 1024;
      __builtin_amdgcn_raw_buffer_store_b128(merge_tiles(hh[0], hh[1]), rsw, ho, 0, 16);
      __builtin_amdgcn_raw_buffer_store_b128(merge_tiles(hh[2], hh[3]), rsw, ho + 144 * 1024, 0, 16);
      SB();
      WAITV(2);
#pragma unroll
      for (int j = 0; j < 8; ++j) { w2f[j][0] = ldtr(lds + slot_addr(t, c, j) + r2); w2f[j][1] = ldtr(lds + slot_addr(t, c, j) + (r2 ^ 64)); }
      u32x2 kf[5], vf[5], qf[4];
      kf[0] = norm_tile(hk, cs[1], st[0]);
#pragma unroll
      for (int i = 0; i < 4; ++i) { kf[i + 1] = norm_tile(a.m[1][i], cs[1], st[i + 1]); qf[i] = norm_tile(a.m[0][i], cs[0], st[i + 1]); }
#pragma unroll
      for (int i = 0; i < 5; ++i) {
        const f32x4 rs = *(LAS f32x4*)(lds + L_RS + (t * 64 + i * 16 + g * 4) * 4), nm = *(LAS f32x4*)(lds + L_NM + (t * 64 + i * 16 + g * 4) * 4);
        const f32x4 v = i ? a.m[2][i ? i - 1 : 0] : hv;
        vf[i] = u32x2{pk(v[0] * rs[0] + nm[0] * csv, v[1] * rs[1] + nm[1] * csv), pk(v[2] * rs[2] + nm[2] * csv, v[3] * rs[3] + nm[3] * csv)};
      }
      SB();
      asm volatile("s_waitcnt lgkmcnt(0)" ::: "memory");
#pragma unroll
      for (int j = 0; j < 16; ++j) glds(k.vW2, k.w2 + (16 + j) * 8192, k.lb + slot_addr(t, c, j >> 1) + (j & 1) * 1024);
      SB();
      const bool par = n16 & 1;
      const bool inB0 = 4 * g + par <= n16, inB1 = 4 * g + 2 + par <= n16;
      const bool first = (panel == 0 && t == 0);
      u32x2 of[4];
#pragma unroll
      for (int i = 0; i < 4; ++i) {
        const f32x4 z = {0.f, 0.f, 0.f, 0.f};
        const f32x4 sa = mfma16k16(kf[i], qf[i], z), sb = mfma16k16(kf[i + 1], qf[i], z);
        float a0 = par ? sa[1] : sa[0], a1 = par ? sa[3] : sa[2];
        if (i == 0 && first) { a0 = -4e9f; a1 = -4e9f; }
        const float l0 = inB0 ? (par ? sb[1] : sb[0]) : a0, l1 = inB1 ? (par ? sb[3] : sb[2]) : a1;
        const float mx = max32(max16(fmaxf(l0, l1)));
        const float SC = 0.25f * 1.4426950408889634f;
        const float p0 = __builtin_amdgcn_exp2f((l0 - mx) * SC), p1 = __builtin_amdgcn_exp2f((l1 - mx) * SC);
        const float inv = __builtin_amdgcn_rcpf(add32(add16(p0 + p1)));
        const u32 pp = pk(p0 * inv, p1 * inv);
        const u32 w0 = par ? (pp << 16) : (pp & 0xffffu), w1 = par ? (pp & 0xffff0000u) : (pp >> 16);
        const u32x4 pf = {inB0 ? 0u : w0, inB1 ? 0u : w1, inB0 ? w0 : 0u, inB1 ? w1 : 0u};
        const u32x4 vv = {vf[i][0], vf[i][1], vf[i + 1][0], vf[i + 1][1]};
        const f32x4 o = mfma16(vv, pf, z);
        of[i] = u32x2{pk(o[0], o[1]), pk(o[2], o[3])};
      }
      const u32 oo = hbase + (4 * idx + c) * 1024;
      __builtin_amdgcn_raw_buffer_store_b128(merge_tiles(of[0], of[1]), rsw, oo, 0, 16);
      __builtin_amdgcn_raw_buffer_store_b128(merge_tiles(of[2], of[3]), rsw, oo + 144 * 1024, 0, 16);
    } else {
      f32x2 st[4];
#pragma unroll
      for (int i = 0; i < 4; ++i) st[i] = *(LAS f32x2*)(lds + L_RN + (16 + t * 64 + ((i * 16) ^ ((c & 1) * 32)) + n16) * 8);
      const u32 s0 = 32 + 32 + 10 * idx;
      const u32 tA = (c & 1) * (144 * 1024), tB = 144 * 1024 - tA;
      const u32 h0 = hbase + (s0 + (idx & 1) * 2 + c) * 1024, hm = hbase + (s0 + ((idx & 1) ? 0 : 8) + (c >> 1)) * 1024;
      u32x2 hh[4];
#pragma unroll
      for (int i = 0; i < 4; ++i) hh[i] = glu_tile(a.m[0][i], a.m[1][i], cs[0], cs[1], st[i]);
      __builtin_amdgcn_raw_buffer_store_b128(merge_tiles(hh[0], hh[1]), rsw, h0 + tA, 0, 16);
      __builtin_amdgcn_raw_buffer_store_b128(merge_tiles(hh[2], hh[3]), rsw, h0 + tB, 0, 16);
      SB();
      WAITV(2);
#pragma unroll
      for (int j = 0; j < 8; ++j) { w2f[j][0] = ldtr(lds + slot_addr(t, c, j) + r2); w2f[j][1] = ldtr(lds + slot_addr(t, c, j) + (r2 ^ 64)); }
#pragma unroll
      for (int i = 0; i < 2; ++i) hh[i] = glu_tile(a.m[4][i], a.m[4][2 + i], cs[4], cs[5], st[i]);
      SB();
      asm volatile("s_waitcnt lgkmcnt(0)" ::: "memory");
#pragma unroll
      for (int j = 0; j < 16; ++j) glds(k.vW2, k.w2 + (16 + j) * 8192, k.lb + slot_addr(t, c, j >> 1) + (j & 1) * 1024);
      SB();
      __builtin_amdgcn_raw_buffer_store_b128(merge_tiles(hh[0], hh[1]), rsw, hm + tA, 0, 16);
#pragma unroll
      for (int i = 0; i < 4; ++i) hh[i] = glu_tile(a.m[2][i], a.m[3][i], cs[2], cs[3], st[i]);
      __builtin_amdgcn_raw_buffer_store_b128(merge_tiles(hh[0], hh[1]), rsw, h0 + 4096 + tA, 0, 16);
      __builtin_amdgcn_raw_buffer_store_b128(merge_tiles(hh[2], hh[3]), rsw, h0 + 4096 + tB, 0, 16);
    }
  }
  STAMP(3);
  SB(); WAITV(0); BAR(); SB();
  STAMP(4);
  const u64 tag = ((u64)thi << 32) | tlo;
  GAS u64* flags = (GAS u64*)(ws + WS_FLAGS);
  if (threadIdx.x == 0) __hip_atomic_store(flags + panel * 16 + slice, tag, __ATOMIC_RELAXED, __HIP_MEMORY_SCOPE_AGENT);
#pragma unroll
  for (int j = 0; j < 8; ++j) { w2f[8 + j][0] = ldtr(lds + slot_addr(t, c, j) + r2); w2f[8 + j][1] = ldtr(lds + slot_addr(t, c, j) + (r2 ^ 64)); }
  SB();
  asm volatile("s_waitcnt lgkmcnt(0)" ::: "memory");
#pragma unroll
  for (int j = 0; j < 6; ++j) glds_if(((1u << (knum - 16)) - 1) & (1u << (j >> 1)), k.vW2, k.w2 + (32 + j) * 8192, k.lb + slot_addr(t, c, j >> 1) + (j & 1) * 1024);
  SB();
  if (w == 0) {
    for (u32 spin = 0; spin < SPIN_CAP; ++spin) {
      const u64 v = __hip_atomic_load(flags + (rb >> 1) * 16 + n16, __ATOMIC_RELAXED, __HIP_MEMORY_SCOPE_AGENT);
      if (__all(v == tag)) break;
      __builtin_amdgcn_s_sleep(1);
    }
  }
  SB(); BAR(); SB();
  STAMP(5);
  const u32 orow = rb * 64 + 4 * w + g, ocol = cb * 128 + n16 * 8;
  u32x2 xres[2];
  {
    const u32 hsrc = WS_H + (rb * 2 * 144 + kbeg) * 1024 + lane * 16;
    constexpr int WIN = 10;
    u32x4 hf[WIN];
    auto hoff = [&](int n) -> u32 { const int tt = n / 19, st = n % 19; const u32 o = hsrc + (tt * 144 + st) * 1024;
                                    return st < 16 ? o : ((u32)st < knum ? o : 0x80000000u); };
#pragma unroll
    for (int n = 0; n < WIN; ++n) hf[n] = __builtin_amdgcn_raw_buffer_load_b128(rsw, hoff(n), 0, 16);
    SB();
    f32x16 acc[2];
#pragma unroll
    for (int n = 0; n < 38; ++n) {
      const int st = n % 19;
      if (st == 0) {
#pragma unroll
        for (int i = 0; i < 16; ++i) { acc[0][i] = 0.f; acc[1][i] = 0.f; }
      }
      if (n == 2) {
#pragma unroll
        for (int j = 0; j < 3; ++j) { w2f[16 + j][0] = ldtr(lds + slot_addr(t, c, j) + r2); w2f[16 + j][1] = ldtr(lds + slot_addr(t, c, j) + (r2 ^ 64)); }
      }
      acc[0] = mfma32(w2f[st][0], hf[n % WIN], acc[0]);
      acc[1] = mfma32(w2f[st][1], hf[n % WIN], acc[1]);
      SB();
      if (n + WIN < 38) hf[n % WIN] = __builtin_amdgcn_raw_buffer_load_b128(rsw, hoff(n + WIN), 0, 16);
      if (n + WIN == 38) {
        xres[0] = *(const GAS u32x2*)(xg + orow * 1024 + ocol);
        xres[1] = *(const GAS u32x2*)(xg + (orow + 32) * 1024 + ocol);
      }
      if (st == 18) {
        const int tt = n / 19;
#pragma unroll
        for (int u = 0; u < 8; ++u) {
          const u32 pos = (lane & 48) | ((lane ^ (2 * u + (lane >> 5))) & 15);
          const f32x4 v = {acc[u >> 2][4 * (u & 3)], acc[u >> 2][4 * (u & 3) + 1], acc[u >> 2][4 * (u & 3) + 2], acc[u >> 2][4 * (u & 3) + 3]};
          *(LAS f32x4*)(lds + slot_addr(t, c, (tt ? 0 : 4) + (u >> 1)) + (u & 1) * 1024 + pos * 16) = v;
        }
      }
      SB();
    }
  }
  if (b == 0 && w == 0) {
    for (u32 spin = 0; spin < SPIN_CAP; ++spin) {
      bool ok = true;
#pragma unroll
      for (int i = 0; i < 4; ++i) ok &= __hip_atomic_load(flags + i * 64 + lane, __ATOMIC_RELAXED, __HIP_MEMORY_SCOPE_AGENT) == tag;
      if (__all(ok)) break;
      __builtin_amdgcn_s_sleep(1);
    }
    if (lane == 0) __hip_atomic_store((GAS u64*)(ws + WS_EPOCH), tag, __ATOMIC_RELAXED, __HIP_MEMORY_SCOPE_AGENT);
  }
  STAMP(6);
  SB(); BAR(); SB();
  asm volatile("" : "+v"(lane));
  g = lane >> 4; n16 = lane & 15;
  {
    const u32 u = n16 >> 1, hh = n16 & 1, tok = 4 * w + g;
    const u32 pos = (hh * 32) | (tok & 16) | ((tok ^ n16) & 15);
#pragma unroll
    for (int tt = 0; tt < 2; ++tt) {
      f32x4 p[8];
#pragma unroll
      for (int ww = 0; ww < 8; ++ww) {
        const int j0 = tt ? 0 : 4;
        const u32 j = j0 + (u >> 1);
        const u32 sa = j < 6 ? ((ww >> 2) + 2 * (j / 3)) * 24576 + (ww & 3) * 6144 + (j % 3) * 2048 : 98304 + (ww >> 2) * 16384 + (ww & 3) * 4096 + (j - 6) * 2048;
        p[ww] = *(LAS f32x4*)(lds + sa + (u & 1) * 1024 + pos * 16);
      }
      const f32x4 s = p[0] + p[5], d = ((p[1] + p[2]) + (p[3] + p[4])) + (p[6] + p[7]);
      float r[4];
#pragma unroll
      for (int i = 0; i < 4; ++i) {
        const float dd = rbf(d[i]);
        const float gl = rbf(dd * __builtin_amdgcn_rcpf(1.0f + __builtin_amdgcn_exp2f(dd * -1.4426950408889634f)));
        const float xr = (i & 1) ? hi(xres[tt][i >> 1]) : lo(xres[tt][i >> 1]);
        r[i] = xr + rbf(rbf(s[i]) * gl);
      }
      *(GAS u32x2*)(out + (orow + 32 * tt) * 1024 + ocol) = u32x2{pk(r[0], r[1]), pk(r[2], r[3])};
    }
  }
  STAMP(7);
  { const u64 tgt = stamp[0] + 6000 + (SCI > 0 ? 4 * (stamp[SCI > 0 ? SCI : 0] - stamp[0]) : 0);
    while (__builtin_amdgcn_s_memrealtime() < tgt) __builtin_amdgcn_s_sleep(4); }
  if (lane == 0) {
    u64* sp = (u64*)(ws + WS_STAMP) + (b * 8 + w) * 16;
    for (int i = 0; i < 10; ++i) sp[i] = stamp[i];
    sp[10] = __builtin_amdgcn_s_getreg((31 << 11) | 4);
  }
}

extern "C" void kernel_launch(void* const* d_in, const int* in_sizes, int n_in, void* d_out, int out_size, void* d_ws, size_t ws_size,
                              hipStream_t stream) {
  fused_block<<<256, 512, 0, stream>>>((const char*)d_in[0], (const char*)d_in[1], (const char*)d_in[2], (const char*)d_in[4],
                                       (const char*)d_in[5], (const char*)d_in[6], (char*)d_out, (char*)d_ws);
}
```

```cpp
#include <hip/hip_runtime.h>

typedef unsigned short u16;
typedef unsigned u32;
typedef unsigned long long u64;
typedef short s16x4 __attribute__((ext_vector_type(4)));
typedef __bf16 bf16x8 __attribute__((ext_vector_type(8)));
typedef __bf16 bf16x2 __attribute__((ext_vector_type(2)));
typedef float f32x2 __attribute__((ext_vector_type(2)));
typedef float f32x4 __attribute__((ext_vector_type(4)));
typedef float f32x16 __attribute__((ext_vector_type(16)));
typedef u32 u32x2 __attribute__((ext_vector_type(2)));
typedef u32 u32x4 __attribute__((ext_vector_type(4)));
#define LAS __attribute__((address_space(3)))
#define GAS __attribute__((address_space(1)))
#define DI __device__ __forceinline__
#define SB() __builtin_amdgcn_sched_barrier(0)

#define ROTMODE 4

constexpr u32 WS_EPOCH = 0;
constexpr u32 WS_FLAGS = 4096;
constexpr u32 WS_STATS = 8192;
constexpr u32 WS_H = 65536;
constexpr u32 H_BYTES = 64u * 144u * 1024u;
constexpr u32 WS_STAMP = WS_H + H_BYTES;
constexpr u32 L_EXKV = 128 * 1024;
constexpr u32 L_EXCS = 144 * 1024;
constexpr u32 L_GRAN = 154 * 1024;
constexpr u32 L_RS = 157 * 1024;
constexpr u32 L_NM = L_RS + 576;
constexpr u32 L_RN = L_NM + 576;
constexpr u64 WTAB = 0x301D13003AULL;
constexpr u32 XTAB = 0x0A4427u;
constexpr u32 SPIN_CAP = 1u << 18;

DI f32x4 mfma16(u32x4 a, u32x4 b, f32x4 c) {
  return __builtin_amdgcn_mfma_f32_16x16x32_bf16(__builtin_bit_cast(bf16x8, a), __builtin_bit_cast(bf16x8, b), c, 0, 0, 0);
}
DI f32x4 mfma16k16(u32x2 a, u32x2 b, f32x4 c) {
  return __builtin_amdgcn_mfma_f32_16x16x16bf16_1k(__builtin_bit_cast(s16x4, a), __builtin_bit_cast(s16x4, b), c, 0, 0, 0);
}
DI f32x16 mfma32(u32x4 a, u32x4 b, f32x16 c) {
  return __builtin_amdgcn_mfma_f32_32x32x16_bf16(__builtin_bit_cast(bf16x8, a), __builtin_bit_cast(bf16x8, b), c, 0, 0, 0);
}
DI u32 pk(float a, float b) { f32x2 v = {a, b}; return __builtin_bit_cast(u32, __builtin_convertvector(v, bf16x2)); }
DI float rbf(float a) { return __uint_as_float(pk(a, a) << 16); }
DI float lo(u32 v) { return __uint_as_float(v << 16); }
DI float hi(u32 v) { return __uint_as_float(v & 0xffff0000u); }

DI void glds(u32 voff, const char* sbase, u32 dst) {
  asm volatile("s_mov_b32 m0, %2\n\ts_nop 0\n\tglobal_load_lds_dwordx4 %0, %1" ::"v"(voff), "s"(sbase), "s"(dst) : "memory");
}
DI void glds_sc1(u32 voff, const char* sbase, u32 dst) {
  asm volatile("s_mov_b32 m0, %2\n\ts_nop 0\n\tglobal_load_lds_dwordx4 %0, %1 sc1" ::"v"(voff), "s"(sbase), "s"(dst) : "memory");
}
DI void glds_if(u32 on, u32 voff, const char* sbase, u32 dst) {
  asm volatile("s_cmp_eq_u32 %3, 0\n\ts_cbranch_scc1 1f\n\ts_mov_b32 m0, %2\n\ts_nop 0\n\tglobal_load_lds_dwordx4 %0, %1\n1:" ::"v"(voff),
               "s"(sbase), "s"(dst), "s"(on)
               : "memory", "scc");
}
DI void glds64_if(u32 on, const char* vaddr, u32 dst) {
  asm volatile("s_cmp_eq_u32 %2, 0\n\ts_cbranch_scc1 1f\n\ts_mov_b32 m0, %1\n\ts_nop 0\n\tglobal_load_lds_dwordx4 %0, off\n1:" ::"v"(vaddr),
               "s"(dst), "s"(on)
               : "memory", "scc");
}
#define WAITV(n) asm volatile("s_waitcnt vmcnt(" #n ")" ::: "memory")
#define BAR() asm volatile("s_waitcnt lgkmcnt(0)\n\ts_barrier" ::: "memory")

DI u32x4 ld128(LAS char* p) { return *(LAS u32x4*)p; }
DI u32x4 ldtr(LAS char* p) {
  s16x4 a = __builtin_amdgcn_ds_read_tr16_b64_v4i16((LAS s16x4*)p);
  s16x4 b = __builtin_amdgcn_ds_read_tr16_b64_v4i16((LAS s16x4*)(p + 512));
  u32x2 x = __builtin_bit_cast(u32x2, a), y = __builtin_bit_cast(u32x2, b);
  return u32x4{x[0], x[1], y[0], y[1]};
}
template <int CTRL> DI float dpp(float v) {
  return __int_as_float(__builtin_amdgcn_update_dpp(0, __float_as_int(v), CTRL, 0xf, 0xf, true));
}
DI float xor16(float v) { auto r = __builtin_amdgcn_permlane16_swap(__float_as_uint(v), __float_as_uint(v), false, false);
  return __uint_as_float(r[0] ^ r[1] ^ __float_as_uint(v)); }
DI float add16(float v) { auto r = __builtin_amdgcn_permlane16_swap(__float_as_uint(v), __float_as_uint(v), false, false);
  return __uint_as_float(r[0]) + __uint_as_float(r[1]); }
DI float add32(float v) { auto r = __builtin_amdgcn_permlane32_swap(__float_as_uint(v), __float_as_uint(v), false, false);
  return __uint_as_float(r[0]) + __uint_as_float(r[1]); }
DI float max16(float v) { auto r = __builtin_amdgcn_permlane16_swap(__float_as_uint(v), __float_as_uint(v), false, false);
  return fmaxf(__uint_as_float(r[0]), __uint_as_float(r[1])); }
DI float max32(float v) { auto r = __builtin_amdgcn_permlane32_swap(__float_as_uint(v), __float_as_uint(v), false, false);
  return fmaxf(__uint_as_float(r[0]), __uint_as_float(r[1])); }
DI float sum32lanes(float v) {
  v += dpp<0xB1>(v);
  v += dpp<0x4E>(v);
  v += dpp<0x141>(v);
  v += dpp<0x140>(v);
  return add16(v);
}

#define STAMP(i)

struct Frag { u32x4 x[4]; u32x4 w[6]; u32x4 hx; };
struct Acc { f32x4 m[5][4]; f32x4 e[6]; };

struct Ctx {
  LAS char* lds;
  const char* img[5];
  const char* mixp;
  const char* xrow;
  const char* xhalo;
  const char* w2;
  const char* gran;
  u32 vA, vG, vX, vW2;
  u32 rW, rW4, rW5, rX, rH;
  u32 rot, c, w, lb;
};

template <int TYPE, int T, bool HALO>
DI void read_frags(const Ctx& k, Frag& f, u32 wa, u32 xa, int sub, int part) {
  LAS char* pw = k.lds + (wa + k.rW);
  LAS char* px = k.lds + (xa + (k.rX ^ (sub * 64)));
  switch (part) {
    case 0: f.x[0] = ld128(px); break;
    case 1: f.w[0] = ldtr(pw); break;
    case 2: f.x[1] = ld128(px + 2048); break;
    case 3: f.w[1] = ldtr(pw + 4096); break;
    case 4: if (TYPE == 0) f.x[2] = ld128(px + 4096); else f.x[2] = ld128(k.lds + (xa + ((k.rX ^ 4096) ^ (sub * 64)))); break;
    case 5: f.w[2] = ldtr(pw + 8192); break;
    case 6: if (TYPE == 0) f.x[3] = ld128(px + 6144); else f.x[3] = ld128(k.lds + (xa + ((k.rX ^ 4096) ^ (sub * 64))) + 2048); break;
    case 7: f.w[3] = ldtr(pw + 12288); break;
    case 8: if (TYPE == 0) f.w[4] = ldtr(pw + 16384); else f.w[4] = ldtr(k.lds + (wa + k.rW4)); break;
    case 9: if (TYPE == 1) f.w[5] = ldtr(k.lds + (wa + k.rW5));
            else if (HALO) f.hx = ld128(k.lds + (xa + (k.rH ^ (sub * 64)))); break;
  }
}

template <int TYPE> DI void main_mfma(Acc& a, const Frag& f, int n) {
  const int j = n >> 2, i = n & 3;
  if (TYPE == 0) {
    if (j == 2) a.m[j][i] = mfma16(f.x[i], f.w[j], a.m[j][i]);
    else a.m[j][i] = mfma16(f.w[j], f.x[i], a.m[j][i]);
  } else {
    if (j < 4) a.m[j][i] = mfma16(f.w[j], f.x[i], a.m[j][i]);
    else a.m[4][i] = mfma16(f.w[4 + (i >> 1)], f.x[i & 1], a.m[4][i]);
  }
}

struct Sites { const char* b[8]; u32 d[8]; u32 on45, on6, on7; u32 mixoff; };

template <int TYPE, int T> DI void make_sites(Ctx& k, Sites& q, int s, u32 wfree, u32 xfree) {
  if (s <= 10) {
    const u32 krow = ((s + 5) * 32 + k.rot * 64) & 511;
    const u32 oA = krow * (TYPE == 0 ? 3072u : 3584u), oG = krow * 3584u;
    q.b[0] = k.img[0] + oA; q.b[1] = k.img[1] + oA; q.b[2] = k.img[2] + oA; q.b[3] = k.img[3] + oG; q.b[4] = k.img[4] + oG;
    q.mixoff = oG;
    const u32 xk = ((((s + 5) >> 1) + k.rot) & 7) * 128;
    q.b[5] = k.xrow + xk; q.b[6] = k.xrow + xk + 32768; q.b[7] = k.xhalo + xk;
#pragma unroll
    for (int j = 0; j < 5; ++j) q.d[j] = k.lb + wfree + j * 4096 + k.c * 1024;
    q.d[5] = k.lb + xfree + T * 8192 + k.c * 1024; q.d[6] = q.d[5] + 4096; q.d[7] = k.lb + xfree + 16384 + k.c * 1024;
    q.on45 = 1; q.on6 = 1; q.on7 = ~k.c & 2;
  } else {
    const u32 e = (s - 11) >> 1;
    const u32 dst = k.lb + (e < 2 ? (T + 2 * e) * 24576 + k.c * 6144 : 98304 + T * 16384 + k.c * 4096);
#pragma unroll
    for (int j = 0; j < 6; ++j) { q.b[j] = k.w2 + (6 * e + j) * 8192; q.d[j] = dst + j * 1024; }
    q.mixoff = (6 * e + 4) * 8192;
    q.b[6] = q.b[0]; q.b[7] = q.b[0]; q.d[6] = dst; q.d[7] = dst;
    q.on45 = 2 - e; q.on6 = 0; q.on7 = 0;
  }
}
template <int TYPE, int T> DI void issue_site(const Ctx& k, const Sites& q, int n) {
  const int j = (T == 0) ? n : (n < 2 ? 5 + n : n - 2);
  if (j < 3) glds(TYPE == 0 ? k.vA : k.vG, q.b[j], q.d[j]);
  else if (j == 3) glds(k.vG, q.b[j], q.d[j]);
  else if (j == 4) { if (TYPE == 0) glds_if(q.on45, k.vG, q.b[4], q.d[4]); else glds64_if(q.on45, k.mixp + q.mixoff, q.d[4]); }
  else if (j == 5) glds_if(q.on45, k.vX, q.b[5], q.d[5]);
  else if (j == 6) glds_if(q.on6, k.vX, q.b[6], q.d[6]);
  else if (TYPE == 0 && T == 0) glds_if(q.on7, k.vX, q.b[7], q.d[7]);
}

template <int TYPE, int T, int PAR>
DI void kstep(Ctx& k, Acc& a, const Frag& cur, Frag& nxt, int s, u64& wtab, u32& xtab, u32& wprev, int lane) {
  constexpr bool ISSUE = (T == ((PAR + 1) & 1));
  const u32 wa = ((u32)wtab & 0xff) << 11;
  wtab = (wtab >> 8) | ((wtab & 0xff) << 32);
  if (PAR == 1) xtab = (xtab >> 8) | ((xtab & 0xff) << 16);
  const u32 xa = (xtab & 0xff) << 11;
  const u32 xfree = ((xtab >> 16) & 0xff) << 11;
  Sites q;
  if (ISSUE) {
    if (s == 11 + T) {
      k.vA = k.vW2; k.vG = k.vW2; k.vX = k.vW2; k.mixp = k.w2 + k.vW2;
      if (T == 0 && k.c < 3) glds_sc1(lane * 16 + k.c * 1024, k.gran, k.lb + L_GRAN + k.c * 1024);
    }
    make_sites<TYPE, T>(k, q, s, wprev, xfree);
  }
  wprev = wa;
  SB();
#pragma unroll
  for (int n = 0; n < 20; ++n) {
    main_mfma<TYPE>(a, cur, n);
    if (n < 10
    ) read_frags<TYPE, T, ISSUE>(k, nxt, wa, xa, (PAR + 1) & 1, n);
    if (ISSUE && n >= 3 && (n & 1) && (n - 3) / 2 < 8 - T) { SB(); issue_site<TYPE, T>(k, q, (n - 3) / 2); }
    SB();
  }
  if (!ISSUE) {
    if (TYPE == 0) {
      u32x4 hx = cur.hx;
      const bool one = (lane & 15) == 0;
#pragma unroll
      for (int i = 0; i < 4; ++i) hx[i] = one ? 0x3F803F80u : hx[i];
      a.e[0] = mfma16(cur.w[0], hx, a.e[0]); a.e[1] = mfma16(cur.w[1], hx, a.e[1]); a.e[2] = mfma16(hx, cur.w[2], a.e[2]);
      a.e[3] = mfma16(cur.w[3], hx, a.e[3]); a.e[4] = mfma16(cur.w[4], hx, a.e[4]);
    } else {
      const u32x4 ones = {0x3F803F80u, 0x3F803F80u, 0x3F803F80u, 0x3F803F80u};
#pragma unroll
      for (int j = 0; j < 6; ++j) a.e[j] = mfma16(cur.w[j], ones, a.e[j]);
    }
    SB();
  }
}

template <int TYPE, int T>
DI void kloop(Ctx& k, Acc& a, int lane, const char* xg, char* ws, u32 srow, u32& tlo, u32& thi, u64* stamp) {
  u32x2 ep; u32x4 r0, r1;
  asm volatile("global_load_dwordx2 %0, %1, %2 sc1" : "=v"(ep) : "v"(0u), "s"(ws) : "memory");
  if (T == 1) {
    const u32 o = (srow + (lane >> 5)) * 1024 + (lane & 31) * 16;
    asm volatile("global_load_dwordx4 %0, %2, %3\n\tglobal_load_dwordx4 %1, %2, %3 offset:512" : "=&v"(r0), "=&v"(r1) : "v"(o), "s"(xg) : "memory");
  }
  {
    Sites q;
    make_sites<TYPE, T>(k, q, T - 5, (u32)((WTAB >> (8 * T)) & 0xff) << 11, (XTAB & 0xff) << 11);
    if (T == 0) { issue_site<TYPE, T>(k, q, 5); issue_site<TYPE, T>(k, q, 6); issue_site<TYPE, T>(k, q, 7);
#pragma unroll
      for (int n = 0; n < 5; ++n) issue_site<TYPE, T>(k, q, n);
    } else {
#pragma unroll
      for (int n = 0; n < 7; ++n) issue_site<TYPE, T>(k, q, n);
    }
    make_sites<TYPE, T>(k, q, T - 3, (u32)((WTAB >> (8 * (T + 2))) & 0xff) << 11, ((XTAB >> 8) & 0xff) << 11);
#pragma unroll
    for (int n = 0; n < 8 - T; ++n) issue_site<TYPE, T>(k, q, n);
  }
  if (T == 0) asm volatile("s_waitcnt vmcnt(7)" : "+v"(ep)::"memory");
  else asm volatile("s_waitcnt vmcnt(12)" : "+v"(ep), "+v"(r0), "+v"(r1)::"memory");
  BAR();
  tlo = __builtin_amdgcn_readfirstlane(ep[0]) + 1; thi = __builtin_amdgcn_readfirstlane(ep[1]) + 1;
  Frag f0, f1;
#pragma unroll
  for (int n = 0; n < 10; ++n) read_frags<TYPE, T, T == 0>(k, f0, (u32)(WTAB & 0xff) << 11, (XTAB & 0xff) << 11, 0, n);
  if (T == 0) {
    Sites q;
    make_sites<TYPE, T>(k, q, -1, (u32)((WTAB >> 32) & 0xff) << 11, ((XTAB >> 16) & 0xff) << 11);
#pragma unroll
    for (int n = 0; n < 8; ++n) issue_site<TYPE, T>(k, q, n);
  } else {
    float v[16];
#pragma unroll
    for (int i = 0; i < 4; ++i) { v[2 * i] = lo(r0[i]); v[2 * i + 1] = hi(r0[i]); v[8 + 2 * i] = lo(r1[i]); v[9 + 2 * i] = hi(r1[i]); }
    float s = 0.f;
#pragma unroll
    for (int i = 0; i < 16; ++i) s += v[i];
    const float mean = sum32lanes(s) * (1.f / 512.f);
    float qq = 0.f;
#pragma unroll
    for (int i = 0; i < 16; ++i) { const float d = v[i] - mean; qq += d * d; }
    const float var = rbf(sum32lanes(qq) * (1.f / 512.f));
    const float sd = rbf(__builtin_sqrtf(rbf(var + 1e-5f)));
    const u32x4 g = {__float_as_uint(rbf(mean)), tlo, __float_as_uint(1.0f / sd), thi};
    if ((lane & 31) == 0) {
      const u32 o = WS_STATS + (srow + (lane >> 5)) * 16;
      asm volatile("global_store_dwordx4 %0, %1, %2 sc1\n\ts_nop 1" ::"v"(o), "v"(g), "s"(ws) : "memory");
    }
    WAITV(8);
  }
  STAMP(8);
  u64 wtab = (WTAB >> 8) | ((WTAB & 0xff) << 32);
  u32 xtab = XTAB, wprev = (u32)(WTAB & 0xff) << 11;
#pragma unroll 1
  for (int it = 0; it < 8; ++it) {
    SB(); BAR(); SB();
    kstep<TYPE, T, 0>(k, a, f0, f1, 2 * it, wtab, xtab, wprev, lane);
    if (T == 0) WAITV(6); else WAITV(11);
    SB(); BAR(); SB();
    kstep<TYPE, T, 1>(k, a, f1, f0, 2 * it + 1, wtab, xtab, wprev, lane);
    if (T == 1) asm volatile("s_waitcnt vmcnt(8)\n\ts_cmp_eq_u32 %0, 0\n\ts_cbranch_scc1 1f\n\ts_waitcnt vmcnt(6)\n1:" ::"s"(it) : "memory", "scc");
  }
  if (T == 1) {
#pragma unroll
    for (int j = 0; j < 4; ++j) glds(k.vW2, k.w2 + (12 + j) * 8192, k.lb + 98304 + 16384 + k.c * 4096 + j * 1024);
  }
}

DI float sigm_mul(float g2, float u) {
  return u * __builtin_amdgcn_rcpf(1.0f + __builtin_amdgcn_exp2f(g2));
}
DI u32x4 merge_tiles(u32x2 a, u32x2 b) {
  auto r0 = __builtin_amdgcn_permlane16_swap(a[0], b[0], false, false);
  auto r1 = __builtin_amdgcn_permlane16_swap(a[1], b[1], false, false);
  return u32x4{r0[0], r1[0], r0[1], r1[1]};
}
DI u32x2 glu_tile(f32x4 g, f32x4 u, f32x4 cg, f32x4 cu, f32x2 st) {
  const float NL = -1.4426950408889634f;
  const float ag = st[0] * NL, bg = st[1] * NL;
  float h[4];
#pragma unroll
  for (int r = 0; r < 4; ++r) h[r] = sigm_mul(g[r] * ag + bg * cg[r], u[r] * st[0] + st[1] * cu[r]);
  return u32x2{pk(h[0], h[1]), pk(h[2], h[3])};
}
DI u32x2 norm_tile(f32x4 v, f32x4 cs, f32x2 st) {
  return u32x2{pk(v[0] * st[0] + st[1] * cs[0], v[1] * st[0] + st[1] * cs[1]), pk(v[2] * st[0] + st[1] * cs[2], v[3] * st[0] + st[1] * cs[3])};
}

DI u32 slot_addr(u32 t, u32 c, int j) {
  return j < 6 ? (t + 2 * (j / 3)) * 24576 + c * 6144 + (j % 3) * 2048 : 98304 + t * 16384 + c * 4096 + (j - 6) * 2048;
}

__global__ __launch_bounds__(512, 2) void fused_block(const char* __restrict__ xg, const char* __restrict__ wqkv, const char* __restrict__ wo,
                                                      const char* __restrict__ wg, const char* __restrict__ wu, const char* __restrict__ wd,
                                                      char* __restrict__ out, char* __restrict__ ws) {
  __shared__ __attribute__((aligned(1024))) char smem[163840];
  u64 stamp[12];
  STAMP(0);
  if (blockIdx.x == 0 && threadIdx.x == 0) __hip_atomic_store((GAS u64*)(ws + 2048), __builtin_amdgcn_s_memrealtime(), __ATOMIC_RELAXED, __HIP_MEMORY_SCOPE_AGENT);
  int lane = threadIdx.x & 63;
  const u32 w = __builtin_amdgcn_readfirstlane(threadIdx.x >> 6), t = w >> 2, c = w & 3;
  const u32 b = blockIdx.x, xcd = b & 7, jb = b >> 3;
  const u32 panel = (xcd & 1) * 8 + (jb & 7), sl = jb >> 3, type = sl >> 1, idx = (xcd >> 1) * 2 + (sl & 1);
  const u32 slice = type * 8 + idx;
  const u32 rb = xcd * 4 + (jb & 3), cb = jb >> 2;
  u32 g = lane >> 4, n16 = lane & 15;
  const u32 kbeg = (u32)((0x7D6B105846332000ULL >> (8 * w)) & 0xff);
  const u32 knum = (u32)((0x1312101312131310ULL >> (8 * w)) & 0xff);

  Ctx k;
  k.lds = (LAS char*)smem;
  k.lb = (u32)(size_t)k.lds;
  k.c = c; k.w = w;
  k.rot = ((panel + sl) & 3) * 2;
  {
    const u32 rr = lane >> 3, pos = lane & 7;
    const u32 gcW = pos ^ ((((lane >> 4) & 1) | ((c & 1) << 1)) << 1);
    k.vA = (8 * c + rr) * 3072 + gcW * 16;
    k.vG = (8 * c + rr) * 3584 + gcW * 16;
    k.vX = (8 * c + rr) * 1024 + (pos ^ (4 * (c & 1) + (lane >> 4))) * 16;
    k.vW2 = rr * 1024 + (pos ^ (((lane >> 4) & 1) << 2)) * 16;
    const u32 q4 = n16 >> 2, p4 = lane & 3, f = (q4 >> 1) | ((g & 1) << 1);
    const u32 rowp = (8 * g + q4) * 128 + 8 * p4;
    k.rW = rowp + ((c ^ f) * 32);
    k.rW4 = 16384 + rowp + (((c >> 1) ^ f) * 32);
    k.rW5 = 16384 + rowp + (((2 + (c >> 1)) ^ f) * 32);
    const u32 xl = n16 * 128 + ((g ^ ((n16 >> 1) & 7)) * 16);
    k.rX = t * 8192 + xl + (type ? (c & 1) * 4096 : 0);
    k.rH = 16384 + xl;
    if (type == 0) {
      k.img[0] = wqkv + idx * 128; k.img[1] = k.img[0] + 1024; k.img[2] = k.img[0] + 2048;
      k.img[3] = wg + idx * 128; k.img[4] = wu + idx * 128;
      k.mixp = wg;
    } else {
      const u32 f0 = 1024 + 320 * idx, o0 = f0 + (idx & 1) * 64, om = (idx & 1) ? f0 : f0 + 256;
      k.img[0] = wg + o0; k.img[1] = wu + o0; k.img[2] = wg + o0 + 128; k.img[3] = wu + o0 + 128; k.img[4] = wg;
      k.mixp = (gcW < 4 ? wg : wu) + (om + (gcW & 3) * 16 + (8 * c + rr) * 3584);
    }
    k.xrow = xg + (panel * 128 + t * 64) * 1024;
    k.xhalo = xg + (panel ? panel * 128 - 16 : 0) * 1024;
    k.w2 = (kbeg < 32 ? wo + kbeg * 16384 : wd + (kbeg - 32) * 16384) + cb * 128;
    k.gran = ws + WS_STATS + ((int)panel * 128 - 16) * 16;
  }
  Acc a;
#pragma unroll
  for (int j = 0; j < 5; ++j)
#pragma unroll
    for (int i = 0; i < 4; ++i) a.m[j][i] = f32x4{0.f, 0.f, 0.f, 0.f};
#pragma unroll
  for (int j = 0; j < 6; ++j) a.e[j] = f32x4{0.f, 0.f, 0.f, 0.f};
  u32 tlo, thi;
  const u32 srow = panel * 128 + slice * 8 + c * 2;
  if (type == 0) { if (t == 0) kloop<0, 0>(k, a, lane, xg, ws, srow, tlo, thi, stamp); else kloop<0, 1>(k, a, lane, xg, ws, srow, tlo, thi, stamp); }
  else           { if (t == 0) kloop<1, 0>(k, a, lane, xg, ws, srow, tlo, thi, stamp); else kloop<1, 1>(k, a, lane, xg, ws, srow, tlo, thi, stamp); }
  STAMP(1);
  asm volatile("" : "+v"(lane));
  g = lane >> 4; n16 = lane & 15;
  LAS char* lds = k.lds;
  const __amdgpu_buffer_rsrc_t rsw = __builtin_amdgcn_make_buffer_rsrc((void*)ws, 0, WS_H + H_BYTES, 0x00020000);

  if (w < 3) {
    const u32 tok = w * 64 + lane;
    const bool need = tok < 144 && (tok >= 16 || (panel != 0 && type == 0));
    u32x4 gr = ld128(lds + L_GRAN + tok * 16);
    for (u32 spin = 0; spin < SPIN_CAP; ++spin) {
      const bool ok = !need || (gr[1] == tlo && gr[3] == thi);
      if (__all(ok)) break;
      __builtin_amdgcn_s_sleep(2);
      gr = __builtin_amdgcn_raw_buffer_load_b128(rsw, WS_STATS + (panel * 128 + (need ? tok : 16) - 16) * 16, 0, 16);
    }
    if (tok < 144) {
      const float rs = need ? __uint_as_float(gr[2]) : 0.f, nm = need ? -__uint_as_float(gr[0]) * rs : 0.f;
      *(LAS float*)(lds + L_RS + tok * 4) = rs;
      *(LAS float*)(lds + L_NM + tok * 4) = nm;
      *(LAS f32x2*)(lds + L_RN + tok * 8) = f32x2{rs, nm};
    }
  }
  {
    LAS char* cs = lds + L_EXCS + w * 384;
    if (n16 == 0) {
#pragma unroll
      for (int j = 0; j < 6; ++j) *(LAS f32x4*)(cs + j * 64 + g * 16) = a.e[j];
    }
    if (type == 0) {
      if (g == 0) *(LAS float*)(cs + 5 * 64 + n16 * 4) = a.e[2][0];
      *(LAS f32x4*)(lds + L_EXKV + w * 2048 + lane * 16) = t ? a.e[1] : a.m[1][3];
      *(LAS f32x4*)(lds + L_EXKV + w * 2048 + 1024 + lane * 16) = t ? a.e[2] : a.m[2][3];
    }
  }
  SB(); BAR(); SB();
  STAMP(2);
  u32x4 w2f[19][2];
  const u32 r2 = (lane >> 5) * 1024 + (n16 >> 2) * 128 + (((n16 >> 3) & 1) * 64) + ((lane >> 4) & 1) * 32 + (lane & 3) * 8;
  const u32 hbase = WS_H + (panel * 4 + t * 2) * (144 * 1024) + lane * 16;
  {
    f32x4 cs[6];
    LAS char* c0 = lds + L_EXCS + c * 384 + g * 16;
#pragma unroll
    for (int j = 0; j < 6; ++j) cs[j] = *(LAS f32x4*)(c0 + j * 64) + *(LAS f32x4*)(c0 + 4 * 384 + j * 64);
    if (type == 0) {
      const float csv = *(LAS float*)(lds + L_EXCS + c * 384 + 320 + n16 * 4) + *(LAS float*)(lds + L_EXCS + (c + 4) * 384 + 320 + n16 * 4);
      f32x4 hk = *(LAS f32x4*)(lds + L_EXKV + (w ^ 4) * 2048 + lane * 16), hv = *(LAS f32x4*)(lds + L_EXKV + (w ^ 4) * 2048 + 1024 + lane * 16);
      if (t == 0) { hk += a.e[1]; hv += a.e[2]; }
      f32x2 st[5];
#pragma unroll
      for (int i = 0; i < 5; ++i) st[i] = *(LAS f32x2*)(lds + L_RN + (t * 64 + i * 16 + n16) * 8);
      u32x2 hh[4];
#pragma unroll
      for (int i = 0; i < 4; ++i) hh[i] = glu_tile(a.m[3][i], a.m[4][i], cs[3], cs[4], st[i + 1]);
      const u32 ho = hbase + (32 + 4 * idx + c) * 1024;
      __builtin_amdgcn_raw_buffer_store_b128(merge_tiles(hh[0], hh[1]), rsw, ho, 0, 16);
      __builtin_amdgcn_raw_buffer_store_b128(merge_tiles(hh[2], hh[3]), rsw, ho + 144 * 1024, 0, 16);
      SB();
      WAITV(2);
#pragma unroll
      for (int j = 0; j < 8; ++j) { w2f[j][0] = ldtr(lds + slot_addr(t, c, j) + r2); w2f[j][1] = ldtr(lds + slot_addr(t, c, j) + (r2 ^ 64)); }
      u32x2 kf[5], vf[5], qf[4];
      kf[0] = norm_tile(hk, cs[1], st[0]);
#pragma unroll
      for (int i = 0; i < 4; ++i) { kf[i + 1] = norm_tile(a.m[1][i], cs[1], st[i + 1]); qf[i] = norm_tile(a.m[0][i], cs[0], st[i + 1]); }
#pragma unroll
      for (int i = 0; i < 5; ++i) {
        const f32x4 rs = *(LAS f32x4*)(lds + L_RS + (t * 64 + i * 16 + g * 4) * 4), nm = *(LAS f32x4*)(lds + L_NM + (t * 64 + i * 16 + g * 4) * 4);
        const f32x4 v = i ? a.m[2][i ? i - 1 : 0] : hv;
        vf[i] = u32x2{pk(v[0] * rs[0] + nm[0] * csv, v[1] * rs[1] + nm[1] * csv), pk(v[2] * rs[2] + nm[2] * csv, v[3] * rs[3] + nm[3] * csv)};
      }
      SB();
      asm volatile("s_waitcnt lgkmcnt(0)" ::: "memory");
#pragma unroll
      for (int j = 0; j < 16; ++j) glds(k.vW2, k.w2 + (16 + j) * 8192, k.lb + slot_addr(t, c, j >> 1) + (j & 1) * 1024);
      SB();
      const bool par = n16 & 1;
      const bool inB0 = 4 * g + par <= n16, inB1 = 4 * g + 2 + par <= n16;
      const bool first = (panel == 0 && t == 0);
      u32x2 of[4];
#pragma unroll
      for (int i = 0; i < 4; ++i) {
        const f32x4 z = {0.f, 0.f, 0.f, 0.f};
        const f32x4 sa = mfma16k16(kf[i], qf[i], z), sb = mfma16k16(kf[i + 1], qf[i], z);
        float a0 = par ? sa[1] : sa[0], a1 = par ? sa[3] : sa[2];
        if (i == 0 && first) { a0 = -4e9f; a1 = -4e9f; }
        const float l0 = inB0 ? (par ? sb[1] : sb[0]) : a0, l1 = inB1 ? (par ? sb[3] : sb[2]) : a1;
        const float mx = max32(max16(fmaxf(l0, l1)));
        const float SC = 0.25f * 1.4426950408889634f;
        const float p0 = __builtin_amdgcn_exp2f((l0 - mx) * SC), p1 = __builtin_amdgcn_exp2f((l1 - mx) * SC);
        const float inv = __builtin_amdgcn_rcpf(add32(add16(p0 + p1)));
        const u32 pp = pk(p0 * inv, p1 * inv);
        const u32 w0 = par ? (pp << 16) : (pp & 0xffffu), w1 = par ? (pp & 0xffff0000u) : (pp >> 16);
        const u32x4 pf = {inB0 ? 0u : w0, inB1 ? 0u : w1, inB0 ? w0 : 0u, inB1 ? w1 : 0u};
        const u32x4 vv = {vf[i][0], vf[i][1], vf[i + 1][0], vf[i + 1][1]};
        const f32x4 o = mfma16(vv, pf, z);
        of[i] = u32x2{pk(o[0], o[1]), pk(o[2], o[3])};
      }
      const u32 oo = hbase + (4 * idx + c) * 1024;
      __builtin_amdgcn_raw_buffer_store_b128(merge_tiles(of[0], of[1]), rsw, oo, 0, 16);
      __builtin_amdgcn_raw_buffer_store_b128(merge_tiles(of[2], of[3]), rsw, oo + 144 * 1024, 0, 16);
    } else {
      f32x2 st[4];
#pragma unroll
      for (int i = 0; i < 4; ++i) st[i] = *(LAS f32x2*)(lds + L_RN + (16 + t * 64 + ((i * 16) ^ ((c & 1) * 32)) + n16) * 8);
      const u32 s0 = 32 + 32 + 10 * idx;
      const u32 tA = (c & 1) * (144 * 1024), tB = 144 * 1024 - tA;
      const u32 h0 = hbase + (s0 + (idx & 1) * 2 + c) * 1024, hm = hbase + (s0 + ((idx & 1) ? 0 : 8) + (c >> 1)) * 1024;
      u32x2 hh[4];
#pragma unroll
      for (int i = 0; i < 4; ++i) hh[i] = glu_tile(a.m[0][i], a.m[1][i], cs[0], cs[1], st[i]);
      __builtin_amdgcn_raw_buffer_store_b128(merge_tiles(hh[0], hh[1]), rsw, h0 + tA, 0, 16);
      __builtin_amdgcn_raw_buffer_store_b128(merge_tiles(hh[2], hh[3]), rsw, h0 + tB, 0, 16);
      SB();
      WAITV(2);
#pragma unroll
      for (int j = 0; j < 8; ++j) { w2f[j][0] = ldtr(lds + slot_addr(t, c, j) + r2); w2f[j][1] = ldtr(lds + slot_addr(t, c, j) + (r2 ^ 64)); }
#pragma unroll
      for (int i = 0; i < 2; ++i) hh[i] = glu_tile(a.m[4][i], a.m[4][2 + i], cs[4], cs[5], st[i]);
      SB();
      asm volatile("s_waitcnt lgkmcnt(0)" ::: "memory");
#pragma unroll
      for (int j = 0; j < 16; ++j) glds(k.vW2, k.w2 + (16 + j) * 8192, k.lb + slot_addr(t, c, j >> 1) + (j & 1) * 1024);
      SB();
      __builtin_amdgcn_raw_buffer_store_b128(merge_tiles(hh[0], hh[1]), rsw, hm + tA, 0, 16);
#pragma unroll
      for (int i = 0; i < 4; ++i) hh[i] = glu_tile(a.m[2][i], a.m[3][i], cs[2], cs[3], st[i]);
      __builtin_amdgcn_raw_buffer_store_b128(merge_tiles(hh[0], hh[1]), rsw, h0 + 4096 + tA, 0, 16);
      __builtin_amdgcn_raw_buffer_store_b128(merge_tiles(hh[2], hh[3]), rsw, h0 + 4096 + tB, 0, 16);
    }
  }
  STAMP(3);
  SB(); WAITV(0); BAR(); SB();
  STAMP(4);
  const u64 tag = ((u64)thi << 32) | tlo;
  GAS u64* flags = (GAS u64*)(ws + WS_FLAGS);
  if (threadIdx.x == 0) __hip_atomic_store(flags + panel * 16 + slice, tag, __ATOMIC_RELAXED, __HIP_MEMORY_SCOPE_AGENT);
#pragma unroll
  for (int j = 0; j < 8; ++j) { w2f[8 + j][0] = ldtr(lds + slot_addr(t, c, j) + r2); w2f[8 + j][1] = ldtr(lds + slot_addr(t, c, j) + (r2 ^ 64)); }
  SB();
  asm volatile("s_waitcnt lgkmcnt(0)" ::: "memory");
#pragma unroll
  for (int j = 0; j < 6; ++j) glds_if(((1u << (knum - 16)) - 1) & (1u << (j >> 1)), k.vW2, k.w2 + (32 + j) * 8192, k.lb + slot_addr(t, c, j >> 1) + (j & 1) * 1024);
  SB();
  if (w == 0) {
    for (u32 spin = 0; spin < SPIN_CAP; ++spin) {
      const u64 v = __hip_atomic_load(flags + (rb >> 1) * 16 + n16, __ATOMIC_RELAXED, __HIP_MEMORY_SCOPE_AGENT);
      if (__all(v == tag)) break;
      __builtin_amdgcn_s_sleep(1);
    }
  }
  SB(); BAR(); SB();
  STAMP(5);
  const u32 orow = rb * 64 + 4 * w + g, ocol = cb * 128 + n16 * 8;
  u32x2 xres[2];
  {
    const u32 hsrc = WS_H + (rb * 2 * 144 + kbeg) * 1024 + lane * 16;
    constexpr int WIN = 10;
    u32x4 hf[WIN];
    auto hoff = [&](int n) -> u32 { const int tt = n / 19, st = n % 19; const u32 o = hsrc + (tt * 144 + st) * 1024;
                                    return st < 16 ? o : ((u32)st < knum ? o : 0x80000000u); };
#pragma unroll
    for (int n = 0; n < WIN; ++n) hf[n] = __builtin_amdgcn_raw_buffer_load_b128(rsw, hoff(n), 0, 16);
    SB();
    f32x16 acc[2];
#pragma unroll
    for (int n = 0; n < 38; ++n) {
      const int st = n % 19;
      if (st == 0) {
#pragma unroll
        for (int i = 0; i < 16; ++i) { acc[0][i] = 0.f; acc[1][i] = 0.f; }
      }
      if (n == 2) {
#pragma unroll
        for (int j = 0; j < 3; ++j) { w2f[16 + j][0] = ldtr(lds + slot_addr(t, c, j) + r2); w2f[16 + j][1] = ldtr(lds + slot_addr(t, c, j) + (r2 ^ 64)); }
      }
      acc[0] = mfma32(w2f[st][0], hf[n % WIN], acc[0]);
      acc[1] = mfma32(w2f[st][1], hf[n % WIN], acc[1]);
      SB();
      if (n + WIN < 38) hf[n % WIN] = __builtin_amdgcn_raw_buffer_load_b128(rsw, hoff(n + WIN), 0, 16);
      if (n + WIN == 38) {
        xres[0] = *(const GAS u32x2*)(xg + orow * 1024 + ocol);
        xres[1] = *(const GAS u32x2*)(xg + (orow + 32) * 1024 + ocol);
      }
      if (st == 18) {
        const int tt = n / 19;
#pragma unroll
        for (int u = 0; u < 8; ++u) {
          const u32 pos = (lane & 48) | ((lane ^ (2 * u + (lane >> 5))) & 15);
          const f32x4 v = {acc[u >> 2][4 * (u & 3)], acc[u >> 2][4 * (u & 3) + 1], acc[u >> 2][4 * (u & 3) + 2], acc[u >> 2][4 * (u & 3) + 3]};
          *(LAS f32x4*)(lds + slot_addr(t, c, (tt ? 0 : 4) + (u >> 1)) + (u & 1) * 1024 + pos * 16) = v;
        }
      }
      SB();
    }
  }
  if (b == 0 && w == 0) {
    for (u32 spin = 0; spin < SPIN_CAP; ++spin) {
      bool ok = true;
#pragma unroll
      for (int i = 0; i < 4; ++i) ok &= __hip_atomic_load(flags + i * 64 + lane, __ATOMIC_RELAXED, __HIP_MEMORY_SCOPE_AGENT) == tag;
      if (__all(ok)) break;
      __builtin_amdgcn_s_sleep(1);
    }
    if (lane == 0) __hip_atomic_store((GAS u64*)(ws + WS_EPOCH), tag, __ATOMIC_RELAXED, __HIP_MEMORY_SCOPE_AGENT);
  }
  STAMP(6);
  SB(); BAR(); SB();
  asm volatile("" : "+v"(lane));
  g = lane >> 4; n16 = lane & 15;
  {
    const u32 u = n16 >> 1, hh = n16 & 1, tok = 4 * w + g;
    const u32 pos = (hh * 32) | (tok & 16) | ((tok ^ n16) & 15);
#pragma unroll
    for (int tt = 0; tt < 2; ++tt) {
      f32x4 p[8];
#pragma unroll
      for (int ww = 0; ww < 8; ++ww) {
        const int j0 = tt ? 0 : 4;
        const u32 j = j0 + (u >> 1);
        const u32 sa = j < 6 ? ((ww >> 2) + 2 * (j / 3)) * 24576 + (ww & 3) * 6144 + (j % 3) * 2048 : 98304 + (ww >> 2) * 16384 + (ww & 3) * 4096 + (j - 6) * 2048;
        p[ww] = *(LAS f32x4*)(lds + sa + (u & 1) * 1024 + pos * 16);
      }
      const f32x4 s = p[0] + p[5], d = ((p[1] + p[2]) + (p[3] + p[4])) + (p[6] + p[7]);
      float r[4];
#pragma unroll
      for (int i = 0; i < 4; ++i) {
        const float dd = rbf(d[i]);
        const float gl = rbf(dd * __builtin_amdgcn_rcpf(1.0f + __builtin_amdgcn_exp2f(dd * -1.4426950408889634f)));
        const float xr = (i & 1) ? hi(xres[tt][i >> 1]) : lo(xres[tt][i >> 1]);
        r[i] = xr + rbf(rbf(s[i]) * gl);
      }
      *(GAS u32x2*)(out + (orow + 32 * tt) * 1024 + ocol) = u32x2{pk(r[0], r[1]), pk(r[2], r[3])};
    }
  }
}

__global__ void k_rec(char* ws) { if (threadIdx.x == 0) __hip_atomic_store((GAS u64*)(ws + 2048), __builtin_amdgcn_s_memrealtime(), __ATOMIC_RELAXED, __HIP_MEMORY_SCOPE_AGENT); }
__global__ void k_spin(char* ws) {
  { const u64 tgt = __hip_atomic_load((GAS u64*)(ws + 2048), __ATOMIC_RELAXED, __HIP_MEMORY_SCOPE_AGENT) + 6000;
    while (__builtin_amdgcn_s_memrealtime() < tgt) __builtin_amdgcn_s_sleep(4); }
}
extern "C" void kernel_launch(void* const* d_in, const int* in_sizes, int n_in, void* d_out, int out_size, void* d_ws, size_t ws_size,
                              hipStream_t stream) {
  fused_block<<<256, 512, 0, stream>>>((const char*)d_in[0], (const char*)d_in[1], (const char*)d_in[2], (const char*)d_in[4],
                                       (const char*)d_in[5], (const char*)d_in[6], (char*)d_out, (char*)d_ws);
  k_spin<<<1, 64, 0, stream>>>((char*)d_ws);
}
```

```cpp
#include <hip/hip_runtime.h>

typedef unsigned short u16;
typedef unsigned u32;
typedef unsigned long long u64;
typedef short s16x4 __attribute__((ext_vector_type(4)));
typedef __bf16 bf16x8 __attribute__((ext_vector_type(8)));
typedef __bf16 bf16x2 __attribute__((ext_vector_type(2)));
typedef float f32x2 __attribute__((ext_vector_type(2)));
typedef float f32x4 __attribute__((ext_vector_type(4)));
typedef float f32x16 __attribute__((ext_vector_type(16)));
typedef u32 u32x2 __attribute__((ext_vector_type(2)));
typedef u32 u32x4 __attribute__((ext_vector_type(4)));
#define LAS __attribute__((address_space(3)))
#define GAS __attribute__((address_space(1)))
#define DI __device__ __forceinline__
#define SB() __builtin_amdgcn_sched_barrier(0)

#define ROTMODE 2
#define MIDN 33
#define P2_RB(b) (((b) & 7) * 4 + (((b) >> 3) & 3))
#define P2_CB(b) ((b) >> 5)
#define FLN 8
#define ROT2 1
#define WIN 6

constexpr u32 WS_EPOCH = 0;
constexpr u32 WS_FLAGS = 2048;
constexpr u32 WS_STATS = 8192;
constexpr u32 WS_H = 65536;
constexpr u32 H_BYTES = 64u * 144u * 1024u;
constexpr u32 WS_STAMP = WS_H + H_BYTES;
constexpr u32 L_EXKV = 136 * 1024;
constexpr u32 L_ONES = 154 * 1024;
constexpr u32 L_EXCS = L_ONES + 128;
constexpr u32 L_CNT = L_EXCS + 1536;
constexpr u32 L_GRAN = L_CNT + 256;
constexpr u32 L_RS = L_GRAN + 3072;
constexpr u32 L_NM = L_RS + 576;
static_assert(L_NM + 576 <= 163840, "LDS map");
constexpr u32 SPIN_CAP = 1u << 18;
constexpr u32 WS5[5] = {58, 0, 19, 29, 48};
constexpr u32 XS3[3] = {39, 68, 10};
constexpr u64 tab8(u32 a, u32 b, u32 c, u32 d, u32 e, u32 f, u32 g, u32 h) {
  return (u64)a | ((u64)b << 8) | ((u64)c << 16) | ((u64)d << 24) | ((u64)e << 32) | ((u64)f << 40) | ((u64)g << 48) | ((u64)h << 56);
}

DI f32x4 mfma16(u32x4 a, u32x4 b, f32x4 c) {
  return __builtin_amdgcn_mfma_f32_16x16x32_bf16(__builtin_bit_cast(bf16x8, a), __builtin_bit_cast(bf16x8, b), c, 0, 0, 0);
}
DI f32x4 mfma16k16(u32x2 a, u32x2 b, f32x4 c) {
  return __builtin_amdgcn_mfma_f32_16x16x16bf16_1k(__builtin_bit_cast(s16x4, a), __builtin_bit_cast(s16x4, b), c, 0, 0, 0);
}
DI f32x16 mfma32(u32x4 a, u32x4 b, f32x16 c) {
  return __builtin_amdgcn_mfma_f32_32x32x16_bf16(__builtin_bit_cast(bf16x8, a), __builtin_bit_cast(bf16x8, b), c, 0, 0, 0);
}
DI u32 pk(float a, float b) { f32x2 v = {a, b}; return __builtin_bit_cast(u32, __builtin_convertvector(v, bf16x2)); }
DI float rbf(float a) { return __uint_as_float(pk(a, a) << 16); }
DI float lo(u32 v) { return __uint_as_float(v << 16); }
DI float hi(u32 v) { return __uint_as_float(v & 0xffff0000u); }
DI u32x4 srd(const char* p) { const u64 a = (u64)p; return u32x4{(u32)a, (u32)(a >> 32) & 0xffffu, 0x7fffffffu, 0x00020000u}; }

template <int IMM> DI void dma(u32 voff, u32x4 rs, u32 soff, u32 dst) {
  asm volatile("s_add_u32 m0, %3, %4\n\ts_nop 0\n\tbuffer_load_dwordx4 %0, %1, %2 offen lds" ::"v"(voff), "s"(rs), "s"(soff), "s"(dst), "i"(IMM)
               : "memory", "scc");
}
template <int IMM> DI void dmaw(u32 voff, u32x4 rs, u32 dst) {
  asm volatile("s_add_u32 m0, %2, %3\n\ts_nop 0\n\tbuffer_load_dwordx4 %0, %1, m0 offen lds" ::"v"(voff), "s"(rs), "s"(dst), "i"(IMM) : "memory", "scc");
}
template <int IMM> DI void dmaw_if(u32 skip, u32 voff, u32x4 rs, u32 dst) {
  asm volatile("s_cmp_lg_u32 %4, 0\n\ts_cbranch_scc1 1f\n\ts_add_u32 m0, %2, %3\n\ts_nop 0\n\tbuffer_load_dwordx4 %0, %1, m0 offen lds\n1:" ::"v"(voff), "s"(rs), "s"(dst), "i"(IMM),
               "s"(skip) : "memory", "scc");
}
template <int IMM> DI void dma_if(u32 on, u32 voff, u32x4 rs, u32 soff, u32 dst) {
  asm volatile("s_cmp_eq_u32 %5, 0\n\ts_cbranch_scc1 1f\n\ts_add_u32 m0, %3, %4\n\ts_nop 0\n\tbuffer_load_dwordx4 %0, %1, %2 offen lds\n1:" ::"v"(voff),
               "s"(rs), "s"(soff), "s"(dst), "i"(IMM), "s"(on)
               : "memory", "scc");
}
DI void dma64(const char* vaddr, u32 dst, u32 dst2) {
  asm volatile("s_add_u32 m0, %1, %2\n\ts_nop 0\n\tglobal_load_lds_dwordx4 %0, off" ::"v"(vaddr), "s"(dst), "s"(dst2) : "memory", "scc");
}
#define WAITV(n) asm volatile("s_waitcnt vmcnt(" #n ")" ::: "memory")
#define WAITL() do { __builtin_amdgcn_s_waitcnt(0xC07F); asm volatile("" ::: "memory"); } while (0)
#define BAR() do { SB(); __builtin_amdgcn_s_waitcnt(0xC07F); asm volatile("s_barrier" ::: "memory"); SB(); } while (0)

DI u32x4 ld128(LAS char* p) { return *(LAS u32x4*)p; }
DI u32x4 ldtr(LAS char* p) {
  s16x4 a = __builtin_amdgcn_ds_read_tr16_b64_v4i16((LAS s16x4*)p);
  s16x4 b = __builtin_amdgcn_ds_read_tr16_b64_v4i16((LAS s16x4*)(p + 512));
  u32x2 x = __builtin_bit_cast(u32x2, a), y = __builtin_bit_cast(u32x2, b);
  return u32x4{x[0], x[1], y[0], y[1]};
}
template <int CTRL> DI float dpp(float v) {
  return __int_as_float(__builtin_amdgcn_update_dpp(0, __float_as_int(v), CTRL, 0xf, 0xf, true));
}
DI float add16(float v) { auto r = __builtin_amdgcn_permlane16_swap(__float_as_uint(v), __float_as_uint(v), false, false);
  return __uint_as_float(r[0]) + __uint_as_float(r[1]); }
DI float add32(float v) { auto r = __builtin_amdgcn_permlane32_swap(__float_as_uint(v), __float_as_uint(v), false, false);
  return __uint_as_float(r[0]) + __uint_as_float(r[1]); }
DI float max16(float v) { auto r = __builtin_amdgcn_permlane16_swap(__float_as_uint(v), __float_as_uint(v), false, false);
  return fmaxf(__uint_as_float(r[0]), __uint_as_float(r[1])); }
DI float max32(float v) { auto r = __builtin_amdgcn_permlane32_swap(__float_as_uint(v), __float_as_uint(v), false, false);
  return fmaxf(__uint_as_float(r[0]), __uint_as_float(r[1])); }
DI float sum32lanes(float v) {
  v += dpp<0xB1>(v);
  v += dpp<0x4E>(v);
  v += dpp<0x141>(v);
  v += dpp<0x140>(v);
  return add16(v);
}

#define STAMP(i)

struct Frag { u32x4 x[4]; u32x4 w[6]; u32x4 hx; };
struct Acc { f32x4 m[5][4]; f32x4 e[3]; };

#define ROT 2
struct Ctx {
  LAS char* lds;
  u32x4 rs0, rs1, rsx;
  u64 p2;
  u32 v0, v1, vX;
  u32 so0, so1, sox;
  u32 inc0, inc1, rc;
  u64 inc2e, inc2o;
  u32 wrap2, onh, xh, t, c, w, panel, jb, cb;
  u32 d0, d2, d3;
  u32 m2[16];
};
struct Late {
  u32 vg[4], vst, hl, rho, hbase;
  u32x4 rsw, rs2, gr, sel;
  u32 tlo, thi;
  u64 w2;
  LAS char *csw, *pst, *pex, *prs, *c0, *q[4];
  const char* ws;
  f32x4 st[5], rs[5], nm[5], mA, mA0, mB;
};

#define PRE 9
#define W2PER 3
constexpr int first_seg(int P) {
  if (P >= 56) { const int e = (P - 55) * 8; return e <= 38 ? 12 : e <= 58 ? 13 : e <= 96 ? 14 : e <= 116 ? 15 : 16; }
  const int m = P / 7, r = P % 7, j = 2 * m + (r == 2 || r >= 5);
  return r == 0 ? 2 * m - 4 : j - 4;
}
constexpr int issued(int S) {
  int P = PRE;
  for (int s = 0; s <= S; ++s) for (int q = 0, v = 0; q < 4 && first_seg(P) <= s && v < W2PER; ++q) { v += P >= 56; ++P; }
  return P;
}
constexpr int needed(int S) { return 7 * ((S + 1) >> 1) + (((S + 1) & 1) ? 7 : 5); }
constexpr int NW2 = issued(15) - 56;
struct Bases { LAS char *wa[3], *wb[3], *wc[3], *wd[3], *x[3][2], *y[3][2]; };
template <int P, int TYPE> DI void piece(Ctx& k, const Late& z) {
  SB();
  if constexpr (P >= 56) dmaw<(P - 56) * 8192>(z.vg[(P - 56) >> 2], z.rs2, k.d3);
  else {
    constexpr int m = P / 7, r = P % 7, j = 2 * m + (r == 2 || r >= 5);
    constexpr u32 dW = WS5[j % 5] << 11, dX = XS3[m % 3] << 11;
    if (r == 0) {
      dma<dX>(k.vX, k.rsx, k.sox, k.d3);
      if (TYPE == 0) dma_if<dX + 16384>(k.onh, k.vX, k.rsx, k.sox - k.xh, k.d3);
      k.sox += 128;
    } else if (r < 3) { dma64((const char*)k.p2, k.m2[j], k.d2); k.p2 += (r == 2) ? k.inc2o : k.inc2e; }
    else if (r & 1) { dma<dW>(k.v0, k.rs0, k.so0, k.d0); k.so0 += k.inc0; }
    else { dma<dW + 4096>(k.v1, k.rs1, k.so1, k.d0); k.so1 += k.inc1; }
    if (r == 6 && m < 7 && (m + 1) % (8 / ROT) == 0) {
      const bool wrap = k.rc == ROT - (m + 1) / (8 / ROT);
      k.so0 = wrap ? 0 : k.so0; k.so1 = wrap ? 0 : k.so1; k.sox = wrap ? 16384 : k.sox; k.p2 -= wrap ? k.wrap2 : 0;
    }
  }
  SB();
}

template <int TYPE> DI void main_mfma(Acc& a, const Frag& f, int n) {
  const int j = n >> 2, i = n & 3;
  if (TYPE == 0) {
    if (j == 2) a.m[j][i] = mfma16(f.x[i], f.w[j], a.m[j][i]);
    else a.m[j][i] = mfma16(f.w[j], f.x[i], a.m[j][i]);
  } else {
    if (j < 4) a.m[j][i] = mfma16(f.w[j], f.x[i], a.m[j][i]);
    else a.m[4][i] = mfma16(f.w[4 + (i >> 1)], f.x[i & 1], a.m[4][i]);
  }
}

template <int TYPE> DI void stats_tables(const Ctx& k, const Late& z, u32x4 gr) {
  const u32 o = z.hl + k.w * 1024;
  const bool need = (o < 144 * 16) & ((o >= 256) | (TYPE == 0 && k.panel != 0));
#pragma unroll 1
  for (u32 spin = 0; spin < SPIN_CAP; ++spin) {
    const bool ok = !need | ((gr[1] == z.tlo) & (gr[3] == z.thi));
    if (__builtin_expect(__all(ok), 1)) break;
    __builtin_amdgcn_s_sleep(2);
    asm volatile("buffer_load_dwordx4 %0, %1, %2, %3 offen sc1\n\ts_waitcnt vmcnt(0)" : "=&v"(gr) : "v"(o), "s"(z.rsw), "s"(WS_STATS - 256 + k.panel * 2048) : "memory");
  }
  const u32 ms = need ? gr[0] : 0u;
  const float rs = need ? __uint_as_float(gr[2]) : 0.f, nm = -lo(ms);
  *(LAS f32x4*)(k.lds + L_GRAN + o) = f32x4{nm, rs * -1.4426950408889634f, hi(ms), rs};
  if (TYPE == 0) { if (o < 144 * 16) { *(LAS float*)(k.lds + L_RS + (o >> 2)) = rs; *(LAS float*)(k.lds + L_NM + (o >> 2)) = nm; } }
}

DI LAS char* pin(LAS char* p) { asm volatile("" : "+v"(p)); return p; }
constexpr u32 NEGB = 0xFF61B1E6u;

template <int TYPE, int S> DI void late_setup(const Ctx& k, Late& z, u32 lane) {
  asm volatile("" : "+v"(lane));
  const u32 g = lane >> 4, n16 = lane & 15;
  if (S == 1) {
    const u32 vW2 = (lane >> 3) * 1024 + ((lane & 7) ^ ((g & 1) << 2)) * 16;
#pragma unroll
    for (int i = 0; i < 4; ++i) z.vg[i] = vW2 + (((i + z.rho) & 7) + 3 - i) * 32768;
    asm volatile("" : "+v"(z.vg[0]), "+v"(z.vg[1]), "+v"(z.vg[2]), "+v"(z.vg[3]));
  }
  if (S == 2) {
    const u32 r2 = (lane >> 5) * 8192 + (n16 >> 2) * 128 + (((n16 >> 3) & 1) * 64) + (g & 1) * 32 + (lane & 3) * 8 + k.w * 1024;
    z.q[0] = pin(k.lds + r2); z.q[1] = pin(k.lds + (r2 ^ 64)); z.q[2] = pin(k.lds + (r2 + 65536)); z.q[3] = pin(k.lds + ((r2 ^ 64) + 65536));
  }
  if (S == 3) {
    z.csw = pin(k.lds + (L_EXCS + k.w * 192 + g * 16));
    z.c0 = pin(k.lds + (L_EXCS + k.c * 192 + g * 16));
    z.pst = pin(k.lds + (L_GRAN + (k.t * 64 + n16 + (TYPE ? 16 + (k.c & 1) * 32 : 0)) * 16));
    z.pex = pin(k.lds + (TYPE ? L_GRAN + (k.t * 64 + n16 + 48 - (k.c & 1) * 32) * 16 : L_EXKV + k.c * 2048 + lane * 16));
    z.prs = pin(k.lds + (TYPE ? L_EXCS + (k.c + 4 - k.t * 4) * 192 + g * 16 : L_RS + (k.t * 64 + g * 4) * 4));
  }
  if (S == 4) {
    z.rs2 = srd((const char*)(z.w2 + k.cb * 128 - k.d3 - 3 * 32768));
    z.hbase = WS_H + (k.panel * 4 + k.t * 2) * (144 * 1024);
    asm volatile("" : "+s"(z.rs2), "+s"(z.hbase));
  }
  if (TYPE == 0 && S == 5) {
    const u32 first = (k.panel + k.t) ? 0u : NEGB;
#pragma unroll
    for (int r = 0; r < 4; ++r) {
      const int d = (int)n16 - (int)(4 * g + r);
      const u32 odd = (u32)((d << 31) >> 31), ng = (u32)(d >> 31);
      z.mB[r] = __uint_as_float(NEGB & (odd | ng)); z.mA[r] = __uint_as_float(NEGB & (odd | ~ng));
      z.mA0[r] = __uint_as_float(__float_as_uint(z.mA[r]) | first);
    }
    asm volatile("" : "+v"(z.mA), "+v"(z.mB), "+v"(z.mA0));
  }
  if (TYPE == 0 && S == 6) {
    const u32 sh = (n16 & 1) * 16, s0 = __builtin_amdgcn_alignbit(0x0c0c0100u, 0x0c0c0100u, sh), s1 = __builtin_amdgcn_alignbit(0x0c0c0302u, 0x0c0c0302u, sh);
    const int d = (int)(n16 & 14) - (int)(4 * g);
    const u32 n0 = (u32)(d >> 31), n1 = (u32)((d - 2) >> 31), zz = 0x0c0c0c0cu;
    z.sel = u32x4{(n0 & s0) | (~n0 & zz), (n1 & s1) | (~n1 & zz), (n0 & zz) | (~n0 & s0), (n1 & zz) | (~n1 & s1)};
    asm volatile("" : "+v"(z.sel));
  }
}
template <int N> DI void waitv() { asm volatile("s_waitcnt vmcnt(%0)" ::"i"(N) : "memory"); }

template <int TYPE, int S, int Q> DI void dma_slot(Ctx& k, const Late& z) {
  if constexpr (issued(S - 1) + Q < issued(S)) piece<issued(S - 1) + Q, TYPE>(k, z);
}

template <int TYPE, int S> DI void kstep(Ctx& k, Acc& a, Late& z, const Bases& B, u32 lane, u32& stamp) {
  Frag f;
  constexpr u32 wa = WS5[S % 5] << 11, wi = wa / 59392, wo = wa % 59392, xi = (S >> 1) % 3, sub = S & 1;
  dma_slot<TYPE, S, 0>(k, z);
  LAS char* px = B.x[xi][sub];
  LAS char* px2 = TYPE == 0 ? px + 4096 : B.y[xi][sub];
  LAS char* pa = B.wa[wi] + wo;
  LAS char* pb = B.wb[wi] + wo;
  f.w[0] = ldtr(pa); f.x[0] = ld128(px); f.x[1] = ld128(px + 2048);
  dma_slot<TYPE, S, 1>(k, z);
  f.x[2] = ld128(px2); f.x[3] = ld128(px2 + 2048); f.w[1] = ldtr(pa + 4096);
  dma_slot<TYPE, S, 2>(k, z);
  if (TYPE == 0) {
    f.w[2] = ldtr(B.wc[wi] + wo); f.w[3] = ldtr(pb);
    dma_slot<TYPE, S, 3>(k, z);
    f.w[4] = ldtr(pb + 4096); f.hx = ld128(B.y[xi][sub]);
  } else {
    f.w[2] = ldtr(pb); f.w[3] = ldtr(pb + 4096);
    dma_slot<TYPE, S, 3>(k, z);
    f.w[4] = ldtr(B.wc[wi] + wo); f.w[5] = ldtr(B.wd[wi] + wo);
  }
  SB();
  if constexpr (issued(S) < 56) { asm volatile("" : "+s"(k.so0), "+s"(k.so1), "+s"(k.sox)); asm volatile("" : "+v"(k.p2)); }
  if (S == 7)
    asm volatile("s_cmp_gt_u32 %4, 2\n\ts_cbranch_scc1 1f\n\ts_add_u32 m0, %3, %5\n\ts_nop 0\n\tbuffer_load_dwordx4 %0, %1, %2 offen sc1 lds\n1:" ::"v"(z.hl), "s"(z.rsw),
                 "s"(WS_STATS - 256 + k.panel * 2048 + k.w * 1024), "s"(k.d3), "s"(k.w), "i"(L_GRAN) : "memory", "scc");
  if (S == 12) z.gr = ld128(k.lds + (L_GRAN + z.hl + k.c * 1024));
  if (S == 13) { if (k.w < 3) stats_tables<TYPE>(k, z, z.gr); }
  if (S == 0)
    asm volatile("s_mov_b64 exec, 1\n\tglobal_store_dwordx4 %0, %1, %2 sc1\n\ts_mov_b64 exec, -1\n\ts_nop 0" ::"v"(z.vst), "v"(z.gr), "s"(z.ws) : "memory");
  if constexpr (S < 15) {
    static_assert(issued(S) >= needed(S), "schedule");
    waitv<issued(S) - needed(S) + (needed(S) <= issued(0)) + (needed(S) <= 7)>();
  }
  BAR();
  if (S >= 1 && S <= 6) late_setup<TYPE, S>(k, z, lane);
  if (TYPE == 0) {
    a.e[0] = mfma16(f.w[0], f.hx, a.e[0]); a.e[1] = mfma16(f.w[1], f.hx, a.e[1]); a.e[2] = mfma16(f.hx, f.w[2], a.e[2]);
  } else {
    const u32x4 ones = {0x3F803F80u, 0x3F803F80u, 0x3F803F80u, 0x3F803F80u};
    a.e[0] = mfma16(f.w[0], ones, a.e[0]); a.e[1] = mfma16(f.w[1], ones, a.e[1]); a.e[2] = mfma16(f.w[4], ones, a.e[2]);
  }
#pragma unroll
  for (int n = 0; n < 20; ++n) {
    main_mfma<TYPE>(a, f, n);
    if (S == 15) {
      SB();
      if (TYPE == 0) {
        if (n < 5) z.st[n] = *(LAS f32x4*)(z.pst + n * 256);
        else if (n < 10) z.rs[n - 5] = *(LAS f32x4*)(z.prs + (n - 5) * 64);
        else if (n > 11 && n < 17) z.nm[n - 12] = *(LAS f32x4*)(z.prs + 576 + (n - 12) * 64);
      } else if (n < 4) z.st[n] = *(LAS f32x4*)((n < 2 ? z.pst : z.pex) + (n & 1) * 256);
      SB();
    }
    if (S == 15 && n == 11) {
      SB();
      asm volatile("s_mov_b64 exec, %4\n\tds_write_b128 %0, %1\n\tds_write_b128 %0, %2 offset:64\n\tds_write_b128 %0, %3 offset:128\n\ts_mov_b64 exec, -1" ::"v"(z.csw),
                   "v"(a.e[0]), "v"(a.e[1]), "v"(a.e[2]), "s"(0x0001000100010001ULL) : "memory");
      SB();
    }
  }
  if (S >= 1 && S <= 6) {
#pragma unroll
    for (int n = 0; n < 23; ++n) { __builtin_amdgcn_sched_group_barrier(0x8, 1, 0); if (n < 10) __builtin_amdgcn_sched_group_barrier(0x6, 2, 0); else __builtin_amdgcn_sched_group_barrier(0x6, 1, 0); }
  }
  SB();
  if (S < 15) asm volatile("s_barrier" ::: "memory");
  SB();
  if constexpr (S < 15) kstep<TYPE, S + 1>(k, a, z, B, lane, stamp);
}

template <int TYPE>
DI void kloop(Ctx& k, Acc& a, Late& z, const Bases& B, u32 lane, u32& stamp) {
#pragma unroll
  for (int j = 0; j < 5; ++j)
#pragma unroll
    for (int i = 0; i < 4; ++i) a.m[j][i] = f32x4{0.f, 0.f, 0.f, 0.f};
#pragma unroll
  for (int j = 0; j < 3; ++j) a.e[j] = f32x4{0.f, 0.f, 0.f, 0.f};
  STAMP(9);
  BAR();
  if (k.t) BAR();
  kstep<TYPE, 0>(k, a, z, B, lane, stamp);
}

#define MFMA_SETTLE() asm volatile("s_nop 7")
DI void swap4(f32x4& a, f32x4& b) {
#pragma unroll
  for (int i = 0; i < 4; ++i) { float x = a[i], y = b[i]; asm volatile("v_swap_b32 %0, %1" : "+v"(x), "+v"(y)); a[i] = x; b[i] = y; }
}
DI u32x4 merge_tiles(u32x2 a, u32x2 b) {
  auto r0 = __builtin_amdgcn_permlane16_swap(a[0], b[0], false, false);
  auto r1 = __builtin_amdgcn_permlane16_swap(a[1], b[1], false, false);
  return u32x4{r0[0], r1[0], r0[1], r1[1]};
}
DI u32x2 glu_tile(f32x4 g, f32x4 u, f32x4 cg, f32x4 cu, f32x4 st) {
  const f32x4 gg = (g + cg * st[0]) * st[1], uu = u + cu * st[0];
  f32x4 e;
#pragma unroll
  for (int r = 0; r < 4; ++r) e[r] = __builtin_amdgcn_exp2f(gg[r]);
  const f32x4 den = e * st[2] + st[2];
#pragma unroll
  for (int r = 0; r < 4; ++r) e[r] = __builtin_amdgcn_rcpf(den[r]);
  const f32x4 h = uu * e;
  return u32x2{pk(h[0], h[1]), pk(h[2], h[3])};
}
DI u32x2 norm_tile(f32x4 v, f32x4 cs, float nm, float rs) {
  const f32x4 r = (v + cs * nm) * rs;
  return u32x2{pk(r[0], r[1]), pk(r[2], r[3])};
}
DI float max2(float a, float b) { return __builtin_elementwise_maximum(a, b); }
DI float max3(float a, float b, float c) { return max2(max2(a, b), c); }

struct Args {
  const char* tab[32][4];
  u32 tt[2][24];
  const char* w2[8];
  const char *x, *wo, *wd;
  char *out, *ws;
};

__global__ __launch_bounds__(512, 2) void fused_block(const Args A) {
  __shared__ __attribute__((aligned(1024))) char smem[163840];
  u32 stamp = 0;
  STAMP(0);
  u32 pf, tid = threadIdx.x, b = blockIdx.x;
  const u32 w = __builtin_amdgcn_readfirstlane(tid >> 6), t = w >> 2, c = w & 3;
  const u32 jb = b >> 3, type = b >> 7, idx = (b & 6) + ((b >> 6) & 1), slice = type * 8 + idx;
  const u32 panel = (b & 1) * 8 + (jb & 7);
  const char* const xg = A.x;
  char* const ws = A.ws;
  const u64 s1 = (u64)A.tab[slice * 2 + t][0], s2 = (u64)A.tab[slice * 2 + t][1], s0l = (u64)A.tab[slice * 2 + t][2], s0h = (u64)A.tab[slice * 2 + t][3];
  Ctx k;
  Late z;
  Bases B;
#pragma unroll
  for (int j = 0; j < 16; ++j) k.m2[j] = A.tt[t][j];
  k.inc2e = A.tt[t][16] | ((u64)A.tt[t][17] << 32); k.inc2o = A.tt[t][18] | ((u64)A.tt[t][19] << 32); k.wrap2 = A.tt[t][20];
  {
    u64 pc; u32 len;
    asm volatile("s_getpc_b64 %0\n.Lfb_begin:\n\ts_mov_b32 %1, .Lfb_end-.Lfb_begin" : "=s"(pc), "=s"(len));
    const u32 o = min((jb * 16 + (tid >> 5)) * 128u, len) & ~3u;
    asm volatile("global_load_dword %0, %1, %2" : "=v"(pf) : "v"(o), "s"(pc) : "memory");
  }
  u32 lane = tid & 63;
  u32 g = lane >> 4, n16 = lane & 15;
  const u32 srow = panel * 128 + slice * 8 + w;
  LAS char* const lds = (LAS char*)smem;
  const u32 lb = (u32)(size_t)lds;
  k.lds = lds; k.t = t; k.c = c; k.w = w; k.panel = panel; k.cb = P2_CB(b);
  k.rc = (panel + (jb >> 3)) & (ROT - 1);
  const u32 kk0 = k.rc * (512 / ROT);
  u32x2 ep; u32x4 r0;
  {
    const u32 rr = lane >> 3, pos = lane & 7;
    const u32 gcW = pos ^ (((g & 1) | ((c & 1) << 1)) << 1);
    const u32 row = 8 * c + rr;
    const u32 mul0 = type ? 3584 : 3072, mul1 = (type | t) ? 3584 : 3072;
    z.hl = lane * 16;
    k.vX = (panel * 128 + 8 * w + rr) * 1024 + (pos ^ (4 * (c & 1) + g)) * 16;
    k.v0 = row * mul0 + gcW * 16; k.v1 = row * mul1 + gcW * 16;
    const u32 ow = row * 3584 + gcW * 16;
    u32 o2 = (t ? k.vX : ow) + (t ? 32768 + kk0 * 2 : kk0 * 3584);
    u32 vr = srow * 1024 + z.hl;
    k.sox = kk0 * 2 + 16384; k.so0 = kk0 * mul0; k.so1 = kk0 * mul1; k.inc0 = 32 * mul0; k.inc1 = 32 * mul1;
    k.xh = panel ? 16384 : 0;
    k.onh = (3u >> (w | (type << 1))) & 1;
    k.d3 = lb + w * 1024; k.d2 = lb + c * 1024; k.d0 = k.d2 + t * 8192;
    asm volatile("" : "+v"(k.vX), "+v"(k.v0), "+v"(k.v1), "+v"(o2), "+v"(vr), "+v"(z.hl));
    asm volatile("" : "+s"(k.so0), "+s"(k.so1), "+s"(k.d0), "+s"(k.d2), "+s"(k.d3), "+s"(k.onh), "+s"(k.xh), "+s"(k.sox));
    SB();
    asm volatile("global_load_dwordx4 %0, %1, %2" : "=v"(r0) : "v"(vr), "s"(xg) : "memory");
    asm volatile("global_load_dwordx2 %0, %1, %2 sc1" : "=v"(ep) : "v"(0u), "s"(ws) : "memory");
    k.rsx = srd(xg - 16384);
    piece<0, 0>(k, z);
    k.rs0 = srd((const char*)s1); k.rs1 = srd((const char*)s2);
    piece<3, 0>(k, z); piece<4, 0>(k, z);
    k.p2 = (gcW < 4 ? s0l : s0h) + o2;
    piece<1, 0>(k, z); piece<2, 0>(k, z);
    asm volatile("s_barrier" ::: "memory");
    asm volatile("" : "+v"(lane));
    g = lane >> 4; n16 = lane & 15;
    SB();
    STAMP(8);
    const u32 q4 = n16 >> 2, f = (q4 >> 1) | ((g & 1) << 1);
    const u32 rowp = (8 * g + q4) * 128 + 8 * (lane & 3);
    const u32 rW = rowp + ((c ^ f) * 32), sw = type ? 8192 : 12288;
    const u32 rW4 = rowp + (((type ? (c >> 1) + 2 - 2 * t : c) ^ f) * 32) + (type ? 16384 : 8192);
    const u32 xl = n16 * 128 + ((g ^ (n16 >> 1)) * 16);
    const u32 ox = t * 8192 + (type & c) * 4096;
    const bool one = (n16 | type) == 0;
#pragma unroll
    for (int i = 0; i < 3; ++i) {
      B.wa[i] = pin(lds + (rW + (i * 59392 + (t ? sw : 0)))); B.wb[i] = pin(lds + (rW + (i * 59392 + (t ? 0 : sw))));
      B.wc[i] = pin(lds + (rW4 + i * 59392)); B.wd[i] = pin(lds + ((rW4 ^ 64) + i * 59392));
#pragma unroll
      for (int o = 0; o < 2; ++o) {
        B.x[i][o] = pin(lds + ((xl ^ (o * 64)) + ((XS3[i] << 11) + ox)));
        B.y[i][o] = pin(lds + (one ? L_ONES : (xl ^ (o * 64)) + ((XS3[i] << 11) + (type ? ox ^ 4096 : 16384))));
      }
    }
    *(LAS u32x4*)(lds + L_ONES) = u32x4{0x3F803F80u, 0x3F803F80u, 0x3F803F80u, 0x3F803F80u};
    *(LAS u32x2*)(lds + L_CNT) = u32x2{0u, 0u};
  }
  z.rho = 2 * ((2 * (jb & 1) + k.cb) & 3); z.w2 = (u64)A.w2[w];
  z.ws = ws; z.vst = WS_STATS + srow * 16;
  z.rsw = srd(ws); z.rsw[2] = WS_H + H_BYTES;
  {
    const u32 ln = (b * 8 + w) * 9 + min(lane, 8u);
    u64 ta = (ln < 4096 ? (u64)A.wo : (u64)A.wd - 4096 * 128) + ln * 128;
    asm volatile("" : "+v"(ta), "+v"(z.vst));
    SB();
    piece<5, 0>(k, z); piece<6, 0>(k, z);
    asm volatile("s_waitcnt vmcnt(7)" : "+v"(ep), "+v"(r0), "+v"(pf)::"memory");
    z.tlo = __builtin_amdgcn_readfirstlane(ep[0]) + 1; z.thi = __builtin_amdgcn_readfirstlane(ep[1]) + 1;
    float v[8];
#pragma unroll
    for (int i = 0; i < 4; ++i) { v[2 * i] = lo(r0[i]); v[2 * i + 1] = hi(r0[i]); }
    const float mean = add32(sum32lanes(((v[0] + v[1]) + (v[2] + v[3])) + ((v[4] + v[5]) + (v[6] + v[7])))) * (1.f / 512.f);
    float qq = 0.f;
#pragma unroll
    for (int i = 0; i < 8; ++i) { const float d = v[i] - mean; qq += d * d; }
    const float var = rbf(add32(sum32lanes(qq)) * (1.f / 512.f));
    const float sd = rbf(__builtin_amdgcn_sqrtf(rbf(var + 1e-5f)));
    z.gr = u32x4{pk(mean, sd), z.tlo, __float_as_uint(__builtin_amdgcn_rcpf(sd)), z.thi};
    asm volatile("" : "+v"(z.gr));
    SB();
    asm volatile("s_mov_b32 m0, %1\n\ts_nop 0\n\tglobal_load_lds_dword %0, off" ::"v"(ta), "s"(lb + L_GRAN) : "memory");
    if (PRE > 7) piece<7, 0>(k, z);
    if (PRE > 8) piece<8, 0>(k, z);
    if (PRE > 9) piece<9, 0>(k, z);
    if (PRE > 10) piece<10, 0>(k, z);
    waitv<PRE - 4>();
  }
  const u32 knum = (u32)((0x1312101312131310ULL >> (8 * w)) & 0xff);
  const u32 dw2 = k.d3, rho = z.rho;
  const __amdgpu_buffer_rsrc_t rsw = __builtin_amdgcn_make_buffer_rsrc((void*)ws, 0, WS_H + H_BYTES, 0x00020000);
  u32x4 w2f[19][2];
  auto rdw2 = [&](int dstj, int j) {
    w2f[dstj][0] = ldtr(z.q[(j >> 2) * 2] + (j & 3) * 16384); w2f[dstj][1] = ldtr(z.q[(j >> 2) * 2 + 1] + (j & 3) * 16384);
  };
#define W2A(p) do { SB(); dmaw<(p) * 8192>(z.vg[(p) >> 2], z.rs2, dw2); SB(); } while (0)
#define FLIP(i) z.vg[i] += (((i + rho) & 4) ? (u32)-131072 : 131072u)
  auto serve_a = [&] {
    if (NW2 <= 11) W2A(11);
    if (NW2 <= 12) W2A(12);
    if (NW2 <= 13) W2A(13);
    W2A(14); W2A(15);
    if (knum > 16) {
      const u32 v3 = (z.vg[0] & 0x7fff) + (8 + 3) * 32768;
      dma<131072>(v3, z.rs2, dw2, dw2); dma<131072 + 16384>(v3, z.rs2, dw2 + 8192, dw2);
    }
    FLIP(0); FLIP(1); FLIP(2); FLIP(3);
  };
  auto serve_rest = [&] {
    WAITV(10);
    rdw2(0, 0); rdw2(1, 1); rdw2(2, 2);
    SB(); WAITL();
    W2A(0); W2A(1); W2A(2); W2A(3); W2A(4); W2A(5);
    WAITV(6);
    rdw2(3, 3); rdw2(4, 4); rdw2(5, 5); rdw2(6, 6); rdw2(7, 7);
    SB(); WAITL();
    W2A(6); W2A(7); W2A(8); W2A(9); W2A(10); W2A(11); W2A(12); W2A(13); W2A(14); W2A(15);
  };
#define T0(x) do { u32 t_ = t; asm volatile("" : "+s"(t_)); SB(); if (t_ == 0) { x; } SB(); } while (0)
#define FRESH(j0, j1) _Pragma("unroll") for (int j = j0; j < j1; ++j) asm volatile("" : "=v"(w2f[j][0]), "=v"(w2f[j][1]))
#define HST(v, o) asm volatile("buffer_store_dwordx4 %0, %1, %2, %3 offen sc1\n\ts_nop 1" ::"v"(v), "v"(z.hl), "s"(z.rsw), "s"(o) : "memory")
#define W2T(p) dmaw_if<(p) * 8192>(t, z.vg[(p) >> 2], z.rs2, dw2)
#define STAMP2(i) STAMP(i)
  static_assert(NW2 >= 11 && NW2 <= 14, "pieces left for the epilogue");
  u32 type2 = type;
  if (type != 0) {
    Acc a;
    kloop<1>(k, a, z, B, lane, stamp);
    STAMP(1);
    __builtin_amdgcn_s_setprio(1);
    if (t == 0) { BAR(); __builtin_amdgcn_s_setprio(2); serve_a(); SB(); swap4(a.m[4][0], a.m[4][2]); swap4(a.m[4][1], a.m[4][3]); }
    asm volatile("s_waitcnt lgkmcnt(0)" ::: "memory");
    BAR();
    STAMP(2);
    const f32x4 cs2 = *(LAS f32x4*)(z.prs), cs3 = *(LAS f32x4*)(z.prs + 64), cs4 = *(LAS f32x4*)(z.c0 + 4 * 192 + 128), cs5 = *(LAS f32x4*)(z.c0 + 128);
    FRESH(0, 8);
    u32 t1 = t;
    asm volatile("" : "+s"(t1));
    if (t1) { serve_a(); serve_rest(); }
    const u32 s0 = 32 + 32 + 10 * idx;
    const u32 tA = (c & 1) * (144 * 1024), tB = 144 * 1024 - tA;
    const u32 h0 = z.hbase + (s0 + (idx & 1) * 2 + c + 4 * t) * 1024, h1 = h0 + 4096 - t * 8192, hm = z.hbase + (s0 + ((idx & 1) ? 0 : 8) + (c >> 1)) * 1024;
    SB();
    HST(merge_tiles(glu_tile(a.m[0][0], a.m[1][0], a.e[0], a.e[1], z.st[0]), glu_tile(a.m[0][1], a.m[1][1], a.e[0], a.e[1], z.st[1])), h0 + tA);
    SB();
    HST(merge_tiles(glu_tile(a.m[0][2], a.m[1][2], a.e[0], a.e[1], z.st[2]), glu_tile(a.m[0][3], a.m[1][3], a.e[0], a.e[1], z.st[3])), h0 + tB);
    SB();
    HST(merge_tiles(glu_tile(a.m[4][0], a.m[4][2], cs4, cs5, z.st[0]), glu_tile(a.m[4][1], a.m[4][3], cs4, cs5, z.st[1])), hm + tA);
    SB();
    {
      const u32x4 v0 = merge_tiles(glu_tile(a.m[2][0], a.m[3][0], cs2, cs3, z.st[0]), glu_tile(a.m[2][1], a.m[3][1], cs2, cs3, z.st[1]));
      const u32x4 v1 = merge_tiles(glu_tile(a.m[2][2], a.m[3][2], cs2, cs3, z.st[2]), glu_tile(a.m[2][3], a.m[3][3], cs2, cs3, z.st[3]));
      HST(v0, h1 + tA); HST(v1, h1 + tB);
    }
    SB();
#pragma unroll
    for (int i = 0; i < 3; ++i)
      asm volatile("" : "=v"(B.wa[i]), "=v"(B.wb[i]), "=v"(B.wc[i]), "=v"(B.x[i][0]), "=v"(B.x[i][1]), "=v"(B.y[i][0]), "=v"(B.y[i][1]));
    asm volatile("" : "=v"(k.v0), "=v"(k.v1), "=v"(k.vX), "=v"(k.p2), "=v"(z.gr), "=v"(z.vst));
  }
  asm volatile("" : "+s"(type2));
  if (type2 == 0) {
    Acc a;
    kloop<0>(k, a, z, B, lane, stamp);
    STAMP(1);
    __builtin_amdgcn_s_setprio(1);
    const float csv = __int_as_float(__builtin_amdgcn_ds_bpermute(z.hl >> 2 & 60, __float_as_int(a.e[2][0])));
    if (t == 0) {
      BAR();
      __builtin_amdgcn_s_setprio(2);
      serve_a(); SB();
      *(LAS f32x4*)(z.pex) = a.m[1][3];
      *(LAS f32x4*)(z.pex + 1024) = a.m[2][3];
#pragma unroll
      for (int i = 0; i < 4; ++i) { swap4(a.m[0][i], a.m[3][i]); swap4(a.m[1][i], a.m[4][i]); }
    }
    asm volatile("s_waitcnt lgkmcnt(0)" ::: "memory");
    BAR();
    STAMP(2);
    const f32x4 csq = *(LAS f32x4*)(z.c0), csk = *(LAS f32x4*)(z.c0 + 64);
    u32 t1 = t;
    asm volatile("" : "+s"(t1));
    if (t1) { a.e[1] = *(LAS f32x4*)(z.pex); a.e[2] = *(LAS f32x4*)(z.pex + 1024); serve_a(); }
    const f32x4 csg = *(LAS f32x4*)(z.c0 + 4 * 192), csu = *(LAS f32x4*)(z.c0 + 4 * 192 + 64);
    u32x2 kf[5], vf[5], qf[4];
#pragma unroll
    for (int i = 4; i >= 0; --i) {
      const f32x4 v = ((i ? a.m[2][i ? i - 1 : 0] : a.e[2]) + z.nm[i] * csv) * z.rs[i];
      vf[i] = u32x2{pk(v[0], v[1]), pk(v[2], v[3])};
      asm volatile("" : "+v"(vf[i]));
    }
    SB();
    FRESH(0, 8);
    if (t) serve_rest();
    SB();
    f32x4 sa[4], sb[4];
    kf[0] = norm_tile(a.e[1], csk, z.st[0][0], z.st[0][3]);
#pragma unroll
    for (int i = 0; i < 4; ++i) {
      kf[i + 1] = norm_tile(a.m[4][i], csk, z.st[i + 1][0], z.st[i + 1][3]);
      const f32x4 q = a.m[3][i] + csq * z.st[i + 1][0];
      qf[i] = u32x2{pk(q[0], q[1]), pk(q[2], q[3])};
      sa[i] = mfma16k16(kf[i], qf[i], i ? z.mA : z.mA0); sb[i] = mfma16k16(kf[i + 1], qf[i], z.mB);
      asm volatile("" : "+v"(sa[i]), "+v"(sb[i]));
    }
    SB();
    u32x2 hh[4];
    float l0[4], l1[4], mx[4], m1[4], m2[4], m3[4];
#pragma unroll
    for (int i = 0; i < 4; ++i) {
      l0[i] = max2(max3(sa[i][0], sa[i][1], sb[i][0]), sb[i][1]); l1[i] = max2(max3(sa[i][2], sa[i][3], sb[i][2]), sb[i][3]);
      mx[i] = max2(l0[i], l1[i]);
      m1[i] = __int_as_float(__builtin_amdgcn_ds_swizzle(__float_as_int(mx[i]), 0x401F));
      m2[i] = __int_as_float(__builtin_amdgcn_ds_bpermute((z.hl >> 2) ^ 128, __float_as_int(mx[i])));
      m3[i] = __int_as_float(__builtin_amdgcn_ds_bpermute((z.hl >> 2) ^ 192, __float_as_int(mx[i])));
    }
    hh[0] = glu_tile(a.m[0][0], a.m[1][0], csg, csu, z.st[1]);
    hh[1] = glu_tile(a.m[0][1], a.m[1][1], csg, csu, z.st[2]);
    const u32 oo = z.hbase + (4 * idx + c) * 1024;
    HST(merge_tiles(hh[0], hh[1]), oo + 32 * 1024);
    SB();
    const u32x4 ones = {0x3F803F80u, 0x3F803F80u, 0x3F803F80u, 0x3F803F80u};
    const f32x4 zz = {0.f, 0.f, 0.f, 0.f};
    f32x4 sum[4], ov[4];
#pragma unroll
    for (int i = 0; i < 4; ++i) {
      const float m = max2(max3(mx[i], m1[i], m2[i]), m3[i]), sc = z.st[i + 1][1] * -0.25f;
      const u32 pp = pk(__builtin_amdgcn_exp2f((l0[i] - m) * sc), __builtin_amdgcn_exp2f((l1[i] - m) * sc));
      const u32x4 pf4 = {__builtin_amdgcn_perm(pp, pp, z.sel[0]), __builtin_amdgcn_perm(pp, pp, z.sel[1]), __builtin_amdgcn_perm(pp, pp, z.sel[2]),
                         __builtin_amdgcn_perm(pp, pp, z.sel[3])};
      const u32x4 vv = {vf[i][0], vf[i][1], vf[i + 1][0], vf[i + 1][1]};
      sum[i] = mfma16(ones, pf4, zz);
      ov[i] = mfma16(vv, pf4, zz);
      asm volatile("" : "+v"(sum[i]), "+v"(ov[i]));
    }
    SB();
    hh[2] = glu_tile(a.m[0][2], a.m[1][2], csg, csu, z.st[3]);
    hh[3] = glu_tile(a.m[0][3], a.m[1][3], csg, csu, z.st[4]);
    u32x2 of[4];
#pragma unroll
    for (int i = 0; i < 4; ++i) { const f32x4 o = ov[i] * __builtin_amdgcn_rcpf(sum[i][0]); of[i] = u32x2{pk(o[0], o[1]), pk(o[2], o[3])}; }
    const u32x4 v0 = merge_tiles(of[0], of[1]), v1 = merge_tiles(of[2], of[3]), v2 = merge_tiles(hh[2], hh[3]);
    HST(v0, oo); HST(v1, oo + 144 * 1024); HST(v2, oo + (144 + 32) * 1024);
    SB();
  }
  STAMP2(3);
  asm volatile("" : "+s"(b));
  asm volatile("" : "+v"(lane));
  g = lane >> 4; n16 = lane & 15;
  const u32 rb = P2_RB(b), myflag = (((b & 1) * 8 + ((b >> 3) & 7)) * 2 + t) * 16 + (b >> 7) * 8 + (b & 6) + ((b >> 6) & 1);
  const u32 cb = P2_CB(b), kbeg = (u32)((0x7D6B105846332000ULL >> (8 * w)) & 0xff), hl = z.hl;
  const u64 tag = ((u64)z.thi << 32) | z.tlo;
  GAS u64* flags = (GAS u64*)(ws + WS_FLAGS);
  T0(WAITV(4); rdw2(0, 0); rdw2(1, 1); rdw2(2, 2); rdw2(3, 3); rdw2(4, 4); rdw2(5, 5); rdw2(6, 6); rdw2(7, 7));
  SB(); WAITV(0);
  if (lane == 0) {
    if (__hip_atomic_fetch_add((LAS u32*)(lds + L_CNT + t * 4), 1u, __ATOMIC_RELAXED, __HIP_MEMORY_SCOPE_WORKGROUP) == 3u)
      __hip_atomic_store(flags + myflag, tag, __ATOMIC_RELAXED, __HIP_MEMORY_SCOPE_AGENT);
  }
  STAMP2(4);
  if (t == 0) {
    __builtin_amdgcn_s_setprio(0);
    WAITL();
    W2A(0); W2A(1); W2A(2); W2A(3); W2A(4); W2A(5); W2A(6); W2A(7); W2A(8); W2A(9); W2A(10); W2A(11); W2A(12); W2A(13); W2A(14); W2A(15);
    WAITV(12);
  }
  rdw2(8, 0); rdw2(9, 1);
  SB(); WAITL();
  {
    const u32 v3 = (z.vg[0] & 0x7fff) + (8 + 3) * 32768 + 16384;
    if (knum > 16) { dmaw<0>(v3, z.rs2, dw2); dmaw<8192>(v3, z.rs2, dw2); }
    if (knum > 18) { dmaw<16384>(v3, z.rs2, dw2); dmaw<24576>(v3, z.rs2, dw2); }
  }
  SB();
  auto wait_flags = [&](u32 grp) {
#pragma unroll 1
    for (u32 spin = 0; spin < SPIN_CAP; ++spin) {
      const u64 v = __hip_atomic_load(flags + grp * 16 + n16, __ATOMIC_RELAXED, __HIP_MEMORY_SCOPE_AGENT);
      if (__all(v == tag)) break;
      __builtin_amdgcn_s_sleep(1);
    }
  };
  wait_flags(rb & ~1u);
  __builtin_amdgcn_fence(__ATOMIC_ACQUIRE, "wavefront");
  SB();
  STAMP2(5);
  {
    const u32 hsrc = WS_H + (((rb >> 1) * 4 + (rb & 1)) * 144 + kbeg) * 1024;
    u32x4 hf[WIN];
    auto hload = [&](int n) -> u32x4 { const int tt = n / 19, st = n % 19;
      const u32 o = hsrc + tt * (2 * 144 * 1024) + (st < 16 ? (((st >> 1) + rho) & 7) * 2048 : (st & ~1) * 1024);
      return __builtin_amdgcn_raw_buffer_load_b128(rsw, hl + (st & 1) * 1024, st < 16 ? o : ((u32)st < knum ? o : 0x40000000u), 16); };
#pragma unroll
    for (int n = 0; n < WIN; ++n) hf[n] = hload(n);
    SB();
    const u32 ooff = ((rb >> 1) * 128 + (rb & 1) * 32 + 4 * w + g) * 1024 + cb * 128 + n16 * 8;
    const u32 wpos = w * 1024 + ((lane & 48) | ((lane ^ (lane >> 5)) & 15)) * 16;
    const u32 tok = 4 * w + g;
    const u32 rpos = (n16 >> 1) * 8192 + (((n16 & 1) * 32) | (tok & 16) | ((tok ^ n16) & 15)) * 16;
    u32x2 xres[2];
    u64 f1 = 0;
    f32x4 p[8];
    auto fetch = [&](int tt) {
      LAS char* pr = lds + (rpos + (1 - tt) * 65536);
#pragma unroll
      for (int ww = 0; ww < 8; ++ww) p[ww] = *(LAS f32x4*)(pr + ww * 1024);
    };
    auto finish = [&](int tt) {
      const f32x4 s = p[0] + p[5], d = ((p[1] + p[2]) + (p[3] + p[4])) + (p[6] + p[7]);
      const f32x4 dl = d * -1.4426950408889634f, sd = s * d;
      f32x4 e;
#pragma unroll
      for (int i = 0; i < 4; ++i) e[i] = __builtin_amdgcn_exp2f(dl[i]);
      e += 1.0f;
#pragma unroll
      for (int i = 0; i < 4; ++i) e[i] = __builtin_amdgcn_rcpf(e[i]);
      const f32x4 xr = {lo(xres[tt][0]), hi(xres[tt][0]), lo(xres[tt][1]), hi(xres[tt][1])};
      const f32x4 r = sd * e + xr;
      *(GAS u32x2*)(A.out + ooff + tt * 65536) = u32x2{pk(r[0], r[1]), pk(r[2], r[3])};
    };
    f32x16 acc[2][2];
    auto flush = [&](int tt, int u) {
      const f32x16& s = acc[tt][u >> 2];
      const f32x4 v = {s[4 * (u & 3)], s[4 * (u & 3) + 1], s[4 * (u & 3) + 2], s[4 * (u & 3) + 3]};
      *(LAS f32x4*)(lds + ((wpos ^ (u * 32)) + (1 - tt) * 65536) + u * 8192) = v;
    };
#pragma unroll
    for (int n = 0; n < 38; ++n) {
      const int st = n % 19, tt = n / 19;
      if (st == 0) {
#pragma unroll
        for (int i = 0; i < 16; ++i) { acc[tt][0][i] = 0.f; acc[tt][1][i] = 0.f; }
      }
      if (st < 16 || (u32)st < knum) {
        acc[tt][0] = mfma32(w2f[st][0], hf[n % WIN], acc[tt][0]);
        if (n == 19) { SB(); flush(0, 0); flush(0, 1); flush(0, 2); flush(0, 3); SB(); }
        acc[tt][1] = mfma32(w2f[st][1], hf[n % WIN], acc[tt][1]);
      }
      SB();
      if (n + WIN == 19) {
        if (__builtin_expect(!__all(f1 == tag), 0)) wait_flags(rb | 1u);
        __builtin_amdgcn_fence(__ATOMIC_ACQUIRE, "wavefront");
        SB();
      }
      if (n + WIN < 38) hf[n % WIN] = hload(n + WIN);
      if (n < 6) rdw2(10 + n, 2 + n);
      if (n == 6) { w2f[16][0] = ldtr(z.q[2] + 65536 + (lane >> 5) * 8192); w2f[16][1] = ldtr(z.q[3] + 65536 + (lane >> 5) * 8192); }
      if (n == 7) rdw2(17, 0);
      if (n == 8) rdw2(18, 1);
      if (n == FLN) f1 = __hip_atomic_load(flags + (rb | 1u) * 16 + n16, __ATOMIC_RELAXED, __HIP_MEMORY_SCOPE_AGENT);
      if (n == 14) {
        xres[0] = *(const GAS u32x2*)(xg + ooff);
        xres[1] = *(const GAS u32x2*)(xg + ooff + 65536);
      }
      if (n >= 19 && n < 23) flush(0, n - 15);
      if (n == 30 && b == 0 && w == 0) {
#pragma unroll 1
        for (u32 spin = 0; spin < SPIN_CAP; ++spin) {
          bool ok = true;
#pragma unroll
          for (int i = 0; i < 8; ++i) ok &= __hip_atomic_load(flags + i * 64 + lane, __ATOMIC_RELAXED, __HIP_MEMORY_SCOPE_AGENT) == tag;
          if (__all(ok)) break;
          __builtin_amdgcn_s_sleep(1);
        }
        if (lane == 0) __hip_atomic_store((GAS u64*)(ws + WS_EPOCH), tag, __ATOMIC_RELAXED, __HIP_MEMORY_SCOPE_AGENT);
      }
      if (n == 18) { STAMP2(10); }
      if (n == 33) { STAMP2(11); }
      SB();
    }
#pragma unroll
    for (int u = 0; u < 8; ++u) flush(1, u);
    STAMP2(6);
    BAR();
    fetch(0);
    finish(0);
    fetch(1);
    finish(1);
  }
  asm volatile(".Lfb_end:");
}

extern "C" void kernel_launch(void* const* d_in, const int* in_sizes, int n_in, void* d_out, int out_size, void* d_ws, size_t ws_size,
                              hipStream_t stream) {
  const char *x = (const char*)d_in[0], *wqkv = (const char*)d_in[1], *wg = (const char*)d_in[4], *wu = (const char*)d_in[5];
  Args A;
  for (int i = 0; i < 8; ++i) {
    const char** a0 = A.tab[2 * i]; const char** a1 = A.tab[2 * i + 1]; const char** b0 = A.tab[16 + 2 * i]; const char** b1 = A.tab[17 + 2 * i];
    const int f0 = 1024 + 320 * i, co = f0 + (i & 1) * 64, om = (i & 1) ? f0 : f0 + 256;
    a0[0] = wqkv + i * 128;        a0[1] = wqkv + i * 128 + 1024; a0[2] = wu + i * 128; a0[3] = wu + i * 128;
    a1[0] = wqkv + i * 128 + 2048; a1[1] = wg + i * 128;          a1[2] = x;            a1[3] = x;
    b0[0] = wg + co;               b0[1] = wu + co;               b0[2] = wg + om;      b0[3] = wu + om - 64;
    b1[0] = wg + co + 128;         b1[1] = wu + co + 128;         b1[2] = x;            b1[3] = x;
    const int kb = (int)((0x7D6B105846332000ULL >> (8 * i)) & 0xff);
    A.w2[i] = kb < 32 ? (const char*)d_in[2] + kb * 16384 : (const char*)d_in[6] + (kb - 32) * 16384;
  }
  for (int j = 0; j < 16; ++j) { A.tt[0][j] = (WS5[j % 5] << 11) + 16384; A.tt[1][j] = (XS3[(j >> 1) % 3] << 11) + 8192 + (j & 1) * 4096; }
  A.tt[0][16] = 32 * 3584; A.tt[0][17] = 0; A.tt[0][18] = 32 * 3584; A.tt[0][19] = 0; A.tt[0][20] = 512 * 3584;
  A.tt[1][16] = 32768; A.tt[1][17] = 0; A.tt[1][18] = (u32)(128 - 32768); A.tt[1][19] = 0xffffffffu; A.tt[1][20] = 1024;
  for (int j = 21; j < 24; ++j) { A.tt[0][j] = 0; A.tt[1][j] = 0; }
  A.x = x; A.wo = (const char*)d_in[2]; A.wd = (const char*)d_in[6]; A.out = (char*)d_out; A.ws = (char*)d_ws;
  fused_block<<<256, 512, 0, stream>>>(A);
}
```

```cpp
#define DIAG
#define SCI 0
#include <hip/hip_runtime.h>

typedef unsigned short u16;
typedef unsigned u32;
typedef unsigned long long u64;
typedef short s16x4 __attribute__((ext_vector_type(4)));
typedef __bf16 bf16x8 __attribute__((ext_vector_type(8)));
typedef __bf16 bf16x2 __attribute__((ext_vector_type(2)));
typedef float f32x2 __attribute__((ext_vector_type(2)));
typedef float f32x4 __attribute__((ext_vector_type(4)));
typedef float f32x16 __attribute__((ext_vector_type(16)));
typedef u32 u32x2 __attribute__((ext_vector_type(2)));
typedef u32 u32x4 __attribute__((ext_vector_type(4)));
#define LAS __attribute__((address_space(3)))
#define GAS __attribute__((address_space(1)))
#define DI __device__ __forceinline__
#define SB() __builtin_amdgcn_sched_barrier(0)

#define ROTMODE 2
#define MIDN 33
#define P2_RB(b) (((b) & 7) * 4 + (((b) >> 3) & 3))
#define CBMAP 0x76534210u
#define P2_CB(b) ((CBMAP >> (((b) >> 5) * 4)) & 7)
#define FLN 8
#define ROT2 1
#define WIN 6

constexpr u32 WS_EPOCH = 0;
constexpr u32 WS_FLAGS = 49152;
constexpr u32 WS_STATS = 8192;
constexpr u32 WS_CS = 65536;
constexpr u32 WS_H = 2097152;
constexpr u32 H_BYTES = 64u * 144u * 1024u;
constexpr u32 WS_STAMP = WS_H + H_BYTES;
constexpr u32 L_CS = 154 * 1024;
constexpr u32 L_CNT = L_CS + 1280;
constexpr u32 L_BAD = L_CNT + 128;
constexpr u32 L_GRAN = L_BAD + 128;
constexpr u32 L_RS = L_GRAN + 3072;
constexpr u32 L_NM = L_RS + 592;
static_assert(L_NM + 592 <= 163840, "LDS map");
constexpr u32 SPIN_CAP = 1u << 18;
constexpr u32 WS5[5] = {58, 0, 19, 29, 48};
constexpr u32 XS3[3] = {39, 68, 10};
constexpr u64 tab8(u32 a, u32 b, u32 c, u32 d, u32 e, u32 f, u32 g, u32 h) {
  return (u64)a | ((u64)b << 8) | ((u64)c << 16) | ((u64)d << 24) | ((u64)e << 32) | ((u64)f << 40) | ((u64)g << 48) | ((u64)h << 56);
}

DI f32x4 mfma16(u32x4 a, u32x4 b, f32x4 c) {
  return __builtin_amdgcn_mfma_f32_16x16x32_bf16(__builtin_bit_cast(bf16x8, a), __builtin_bit_cast(bf16x8, b), c, 0, 0, 0);
}
DI f32x4 mfma16k16(u32x2 a, u32x2 b, f32x4 c) {
  return __builtin_amdgcn_mfma_f32_16x16x16bf16_1k(__builtin_bit_cast(s16x4, a), __builtin_bit_cast(s16x4, b), c, 0, 0, 0);
}
DI f32x16 mfma32(u32x4 a, u32x4 b, f32x16 c) {
  return __builtin_amdgcn_mfma_f32_32x32x16_bf16(__builtin_bit_cast(bf16x8, a), __builtin_bit_cast(bf16x8, b), c, 0, 0, 0);
}
DI u32 pk(float a, float b) { f32x2 v = {a, b}; return __builtin_bit_cast(u32, __builtin_convertvector(v, bf16x2)); }
DI float rbf(float a) { return __uint_as_float(pk(a, a) << 16); }
DI float lo(u32 v) { return __uint_as_float(v << 16); }
DI float hi(u32 v) { return __uint_as_float(v & 0xffff0000u); }
DI u32x4 srd(const char* p) { const u64 a = (u64)p; return u32x4{(u32)a, (u32)(a >> 32) & 0xffffu, 0x7fffffffu, 0x00020000u}; }

template <int IMM> DI void dma(u32 voff, u32x4 rs, u32 soff, u32 dst) {
  asm volatile("s_add_u32 m0, %3, %4\n\ts_nop 0\n\tbuffer_load_dwordx4 %0, %1, %2 offen lds" ::"v"(voff), "s"(rs), "s"(soff), "s"(dst), "i"(IMM)
               : "memory", "scc");
}
template <int IMM> DI void dmaw(u32 voff, u32x4 rs, u32 dst) {
  asm volatile("s_add_u32 m0, %2, %3\n\ts_nop 0\n\tbuffer_load_dwordx4 %0, %1, m0 offen lds" ::"v"(voff), "s"(rs), "s"(dst), "i"(IMM) : "memory", "scc");
}
template <int IMM> DI void dmaw_if(u32 skip, u32 voff, u32x4 rs, u32 dst) {
  asm volatile("s_cmp_lg_u32 %4, 0\n\ts_cbranch_scc1 1f\n\ts_add_u32 m0, %2, %3\n\ts_nop 0\n\tbuffer_load_dwordx4 %0, %1, m0 offen lds\n1:" ::"v"(voff), "s"(rs), "s"(dst), "i"(IMM),
               "s"(skip) : "memory", "scc");
}
template <int IMM> DI void dma_if(u32 on, u32 voff, u32x4 rs, u32 soff, u32 dst) {
  asm volatile("s_cmp_eq_u32 %5, 0\n\ts_cbranch_scc1 1f\n\ts_add_u32 m0, %3, %4\n\ts_nop 0\n\tbuffer_load_dwordx4 %0, %1, %2 offen lds\n1:" ::"v"(voff),
               "s"(rs), "s"(soff), "s"(dst), "i"(IMM), "s"(on)
               : "memory", "scc");
}
DI void dma64(const char* vaddr, u32 dst, u32 dst2) {
  asm volatile("s_add_u32 m0, %1, %2\n\ts_nop 0\n\tglobal_load_lds_dwordx4 %0, off" ::"v"(vaddr), "s"(dst), "s"(dst2) : "memory", "scc");
}
#define WAITV(n) asm volatile("s_waitcnt vmcnt(" #n ")" ::: "memory")
#define WAITL() do { __builtin_amdgcn_s_waitcnt(0xC07F); asm volatile("" ::: "memory"); } while (0)
#define BAR() do { SB(); __builtin_amdgcn_s_waitcnt(0xC07F); asm volatile("s_barrier" ::: "memory"); SB(); } while (0)

DI u32x4 ld128(LAS char* p) { return *(LAS u32x4*)p; }
DI u32x4 ldtr(LAS char* p) {
  s16x4 a = __builtin_amdgcn_ds_read_tr16_b64_v4i16((LAS s16x4*)p);
  s16x4 b = __builtin_amdgcn_ds_read_tr16_b64_v4i16((LAS s16x4*)(p + 512));
  u32x2 x = __builtin_bit_cast(u32x2, a), y = __builtin_bit_cast(u32x2, b);
  return u32x4{x[0], x[1], y[0], y[1]};
}
template <int CTRL> DI float dpp(float v) {
  return __int_as_float(__builtin_amdgcn_update_dpp(0, __float_as_int(v), CTRL, 0xf, 0xf, true));
}
DI float add16(float v) { auto r = __builtin_amdgcn_permlane16_swap(__float_as_uint(v), __float_as_uint(v), false, false);
  return __uint_as_float(r[0]) + __uint_as_float(r[1]); }
DI float add32(float v) { auto r = __builtin_amdgcn_permlane32_swap(__float_as_uint(v), __float_as_uint(v), false, false);
  return __uint_as_float(r[0]) + __uint_as_float(r[1]); }
DI float max16(float v) { auto r = __builtin_amdgcn_permlane16_swap(__float_as_uint(v), __float_as_uint(v), false, false);
  return fmaxf(__uint_as_float(r[0]), __uint_as_float(r[1])); }
DI float max32(float v) { auto r = __builtin_amdgcn_permlane32_swap(__float_as_uint(v), __float_as_uint(v), false, false);
  return fmaxf(__uint_as_float(r[0]), __uint_as_float(r[1])); }
DI float sum32lanes(float v) {
  v += dpp<0xB1>(v);
  v += dpp<0x4E>(v);
  v += dpp<0x141>(v);
  v += dpp<0x140>(v);
  return add16(v);
}

#define STAMP(i) do { SB(); u64 t_, c_; asm volatile("s_memrealtime %0\n\ts_memtime %1\n\ts_waitcnt lgkmcnt(0)" : "=&s"(t_), "=&s"(c_)::"memory"); \
  asm volatile("v_writelane_b32 %0, %1, " #i "\n\tv_writelane_b32 %0, %2, 16+" #i : "+v"(stamp) : "s"((u32)t_), "s"((u32)c_)); SB(); } while (0)

struct Frag { u32x4 x[4]; u32x4 w[6]; u32x4 hx; };
struct Acc { f32x4 m[5][4]; f32x4 e[2]; f32x4 cs[6]; u32x4 ones; };

#define ROT 4
struct Ctx {
  LAS char* lds;
  u32x4 rs0, rs1, rsx;
  u64 p2;
  u32 v0, v1, vX;
  u32 so0, so1, sox;
  u32 inc0, inc1, rc;
  u64 inc2e, inc2o, w2o;
  u32 w0, w1, wx;
  u32 wrap2, onh, xh, t, c, w, panel, slice, cb, q4, knum;
  u32 d0, d2, d3;
  u32 m2[16];
};
struct Late {
  u32 vg[4], vst, hl, rho, hbase;
  u32x4 rsw, rs2, gr, sel;
  u32 tlo, thi;
  u32 v3t, need, badv, g1, g2, hs, hm;
  u64 w2;
  LAS char *pb, *pg, *pp, *pcw, *pc4, *pst, *pex, *prs, *pc, *q[4];
  const char* ws;
  f32x4 st[5], rs[5], nm[5], cs[6], mA, mA0, mB, tv;
  u32x4 cp[4];
  u32 sc;
  f32x2 csum;
  float csv;
  u64 ta;
};

#define CSL 10
#define T3S 15
#define PSLEEP 1
#define THR 63
#define ORD 2
#define TCH 8
#define PRE 9
#define W2PER 3
constexpr int first_seg(int P) {
  if (P >= 56) { const int e = (P - 55) * 8; return e <= 38 ? 12 : e <= 58 ? 13 : e <= 96 ? 14 : e <= 116 ? 15 : 16; }
  const int m = P / 7, r = P % 7, j = 2 * m + (r == 2 || r >= 5);
  return r == 0 ? 2 * m - 4 : j - 4;
}
constexpr int issued(int S) {
  int P = PRE;
  for (int s = 0; s <= S; ++s) for (int q = 0, v = 0; q < 4 && first_seg(P) <= s && v < W2PER; ++q) { v += P >= 56; ++P; }
  return P;
}
constexpr int needed(int S) { return 7 * ((S + 1) >> 1) + (((S + 1) & 1) ? 7 : 5); }
constexpr int NW2 = issued(15) - 56;
constexpr bool wraps(int m) { return m < 7 && (m + 1) % (8 / ROT) == 0; }
template <int M> DI void set_wrap(Ctx& k) {
  const bool wrap = k.rc == ROT - (M + 1) / (8 / ROT);
  k.w0 = wrap ? k.inc0 * (u32)-15 : k.inc0; k.w1 = wrap ? k.inc1 * (u32)-15 : k.inc1; k.wx = wrap ? (u32)-896 : 128u;
  k.w2o = k.inc2o - (wrap ? k.wrap2 : 0);
  asm volatile("" : "+s"(k.w0), "+s"(k.w1), "+s"(k.wx), "+s"(k.w2o));
}
constexpr int wrap_in(int S) {
  for (int m = 1; m < 7; ++m) if (wraps(m) && 7 * m >= issued(S) && 7 * m < issued(S + 1)) return m;
  return -1;
}
struct Bases { LAS char *wa[3], *wc[3], *wd[3], *x[3][2], *y[3][2]; };
static_assert(ROT == 4, "the column sums are handed over between four rotation classes");
template <int P, int TYPE> DI void piece(Ctx& k, const Late& z) {
  SB();
  if constexpr (P >= 56) dmaw<(P - 56) * 8192>(z.vg[(P - 56) >> 2], z.rs2, k.d3);
  else {
    constexpr int m = P / 7, r = P % 7, j = 2 * m + (r == 2 || r >= 5);
    constexpr u32 dW = WS5[j % 5] << 11, dX = XS3[m % 3] << 11;
    constexpr bool W = wraps(m);
    if (r == 0) {
      dma<dX>(k.vX, k.rsx, k.sox, k.d3);
      if (TYPE == 0) dma_if<dX + 16384>(k.onh, k.vX, k.rsx, k.sox - k.xh, k.d3);
      k.sox += W ? k.wx : 128;
    } else if (r < 3) { dma64((const char*)k.p2, k.m2[j], k.d2); k.p2 += (r == 2) ? (W ? k.w2o : k.inc2o) : k.inc2e; }
    else if (r & 1) { dma<dW>(k.v0, k.rs0, k.so0, k.d0); k.so0 += (W && r == 5) ? k.w0 : k.inc0; }
    else { dma<dW + 4096>(k.v1, k.rs1, k.so1, k.d0); k.so1 += (W && r == 6) ? k.w1 : k.inc1; }
  }
  SB();
}

template <int TYPE> DI void main_mfma(Acc& a, const Frag& f, int n) {
  const int j = n >> 2, i = n & 3;
  if (TYPE == 0) {
    if (j == 2) a.m[j][i] = mfma16(f.x[i], f.w[j], a.m[j][i]);
    else a.m[j][i] = mfma16(f.w[j], f.x[i], a.m[j][i]);
  } else {
    if (j < 4) a.m[j][i] = mfma16(f.w[j], f.x[i], a.m[j][i]);
    else a.m[4][i] = mfma16(f.w[4 + (i >> 1)], f.x[i & 1], a.m[4][i]);
  }
}

DI u32 gran_off(const Ctx& k, const Late& z) { return z.hl + min(k.c, 2u) * 1024; }
template <int TYPE> DI bool gran_need(const Ctx& k, u32 o) { return (o < 144 * 16) & ((o >= 256) | (TYPE == 0 && k.panel != 0)); }
DI f32x4 stats_value(u32 need, u32x4 gr) {
  const u32 ms = need & gr[0];
  const float rs = __uint_as_float(need & gr[2]);
  return f32x4{-lo(ms), rs * -1.4426950408889634f, hi(ms), rs};
}
template <int TYPE> DI void stats_write(const Late& z, f32x4 v) {
  *(LAS f32x4*)(z.pg) = v;
  if (TYPE == 0) { *(LAS float*)z.pp = v[3]; *(LAS float*)(z.pp + L_NM - L_RS) = v[0]; }
}
DI u32 cs_bad(const Late& z) {
  u32 d = 0;
#pragma unroll
  for (int i = 0; i < 4; ++i) d |= (z.cp[i][1] ^ z.tlo) | (z.cp[i][3] ^ z.thi);
  return d;
}
DI f32x2 cs_sum(const Late& z) {
  return f32x2{(__uint_as_float(z.cp[0][0]) + __uint_as_float(z.cp[1][0])) + (__uint_as_float(z.cp[2][0]) + __uint_as_float(z.cp[3][0])),
               (__uint_as_float(z.cp[0][2]) + __uint_as_float(z.cp[1][2])) + (__uint_as_float(z.cp[2][2]) + __uint_as_float(z.cp[3][2]))};
}
#define CS_LOADS "buffer_load_dwordx4 %0, %4, %5, %6 offen sc1\n\tbuffer_load_dwordx4 %1, %4, %5, %7 offen sc1\n\t" \
                 "buffer_load_dwordx4 %2, %4, %5, %8 offen sc1\n\tbuffer_load_dwordx4 %3, %4, %5, %9 offen sc1\n"
template <int TYPE> DI void stats_slow(const Ctx& k, Late& z) {
  const u32 o = gran_off(k, z);
  u32x4 gr;
#pragma unroll 1
  for (u32 spin = 0; spin < SPIN_CAP; ++spin) {
    asm volatile("buffer_load_dwordx4 %0, %1, %2, %3 offen sc1\n\ts_waitcnt vmcnt(0)" : "=&v"(gr) : "v"(o), "s"(z.rsw), "s"(WS_STATS - 256 + k.panel * 2048) : "memory");
    asm volatile(CS_LOADS "\ts_waitcnt vmcnt(0)" : "=&v"(z.cp[0]), "=&v"(z.cp[1]), "=&v"(z.cp[2]), "=&v"(z.cp[3])
                 : "v"(z.hl), "s"(z.rsw), "s"(z.sc), "s"(z.sc + 4096), "s"(z.sc + 8192), "s"(z.sc + 12288) : "memory");
    if (__all(((z.need & ((gr[1] ^ z.tlo) | (gr[3] ^ z.thi))) | cs_bad(z)) == 0)) break;
    __builtin_amdgcn_s_sleep(2);
  }
  stats_write<TYPE>(z, stats_value(z.need, gr)); z.csum = cs_sum(z);
}

DI LAS char* pin(LAS char* p) { asm volatile("" : "+v"(p)); return p; }
constexpr u32 NEGB = 0xFF61B1E6u;

template <int TYPE> DI void late_setup(const Ctx& k, Late& z, u32 lane) {
  asm volatile("" : "+v"(lane));
  const u32 g = lane >> 4, n16 = lane & 15;
  const u32 vW2 = (lane >> 3) * 1024 + ((lane & 7) ^ ((g & 1) << 2)) * 16;
#pragma unroll
  for (int i = 0; i < 4; ++i) z.vg[i] = vW2 + (((i + z.rho) & 7) + 3 - i) * 32768;
  asm volatile("" : "+v"(z.vg[0]), "+v"(z.vg[1]), "+v"(z.vg[2]), "+v"(z.vg[3]));
  const u32 r2 = (lane >> 5) * 8192 + (n16 >> 2) * 128 + (((n16 >> 3) & 1) * 64) + (g & 1) * 32 + (lane & 3) * 8 + k.w * 1024;
  z.q[0] = pin(k.lds + r2); z.q[1] = pin(k.lds + (r2 ^ 64));
  z.pst = pin(k.lds + (L_GRAN + (k.t * 64 + n16 + (TYPE ? 16 + (k.c & 1) * 16 : 0)) * 16));
  z.pex = pin(k.lds + (TYPE ? L_GRAN + (k.t * 64 + n16 + 32 - (k.c & 1) * 16) * 16 : L_CS + k.c * 320 + 128 + n16 * 4));
  const u32 o = gran_off(k, z);
  z.pg = pin(k.lds + (L_GRAN + o));
  if (TYPE == 0) z.pp = pin(k.lds + L_RS + min(o >> 2, 576u));
  z.pb = pin(k.lds + (L_BAD + (lane & 31) * 4));
  z.v3t = 3 * 32768 + min(256 + k.q4 * 12 + min(lane, 11u), k.knum * 16 - 1) * 1024;
  asm volatile("" : "+v"(z.v3t));
  z.need = gran_need<TYPE>(k, o) ? ~0u : 0u;
  z.pcw = pin(k.lds + (L_CS + k.c * 320 + lane * 4));
  z.pc4 = pin(k.lds + (L_CS + k.c * 320 + 256 + n16 * 4));
  z.pc = pin(k.lds + (L_CS + k.c * 320 + g * 16));
  z.prs = pin(k.lds + (TYPE ? L_CS + (k.c & 2) * 320 + 256 + g * 16 : L_RS + (k.t * 64 + g * 4) * 4));
  if (TYPE) { z.hs = z.hl ^ ((k.c & 1) * 256); z.hm = (g & 1) * (144 * 1024) + (g >> 1) * 512 + (k.c & 1) * 256 + n16 * 16; asm volatile("" : "+v"(z.hs), "+v"(z.hm)); }
  z.g1 = (g & 1) ? ~0u : 0u; z.g2 = (g & 2) ? ~0u : 0u;
  asm volatile("" : "+v"(z.need), "+v"(z.g1), "+v"(z.g2));
  z.rs2 = srd((const char*)(z.w2 + k.cb * 128 - k.d3 - 3 * 32768));
  z.hbase = WS_H + (k.panel * 4 + k.t * 2) * (144 * 1024);
  z.sc = WS_CS + (k.slice * 16 + (k.panel & 12)) * 4096 + k.c * 1024;
  asm volatile("" : "+s"(z.rs2), "+s"(z.hbase), "+s"(z.sc));
  if (TYPE == 0) {
#pragma unroll
    for (int r = 0; r < 4; ++r) {
      const int d = (int)n16 - (int)(4 * g + r);
      const u32 odd = (u32)((d << 31) >> 31), ng = (u32)(d >> 31);
      z.mB[r] = __uint_as_float(NEGB & (odd | ng)); z.mA[r] = __uint_as_float(NEGB & (odd | ~ng));
    }
    const u32 sh = (n16 & 1) * 16, s0 = __builtin_amdgcn_alignbit(0x0c0c0100u, 0x0c0c0100u, sh), s1 = __builtin_amdgcn_alignbit(0x0c0c0302u, 0x0c0c0302u, sh);
    const int d = (int)(n16 & 14) - (int)(4 * g);
    const u32 n0 = (u32)(d >> 31), n1 = (u32)((d - 2) >> 31), zz = 0x0c0c0c0cu;
    z.sel = u32x4{(n0 & s0) | (~n0 & zz), (n1 & s1) | (~n1 & zz), (n0 & zz) | (~n0 & s0), (n1 & zz) | (~n1 & s1)};
    asm volatile("" : "+v"(z.mA), "+v"(z.mB), "+v"(z.sel));
  }
}
DI u32 bsel(u32 m, float a, float b) { return (m & __float_as_uint(a)) | (~m & __float_as_uint(b)); }
template <int TYPE, int S> DI void gap_work(Ctx& k, const Acc& a, Late& z) {
  if constexpr (wrap_in(S) >= 0) { set_wrap<wrap_in(S)>(k); }
  if (S == 4) {
    const u32 lo2 = bsel(z.g1, a.cs[1][0], a.cs[0][0]), hi2 = bsel(z.g1, a.cs[3][0], a.cs[2][0]);
    z.gr = u32x4{(z.g2 & hi2) | (~z.g2 & lo2), z.tlo, TYPE ? bsel(0u - (k.c & 1), a.cs[5][0], a.cs[4][0]) : __float_as_uint(a.cs[4][0]), z.thi};
    asm volatile("" : "+v"(z.gr));
  }
  if (S == 12) {
    z.badv = z.need & ((z.gr[1] ^ z.tlo) | (z.gr[3] ^ z.thi));
    z.tv = stats_value(z.need, z.gr);
    asm volatile("" : "+v"(z.tv), "+v"(z.badv));
  }
  if (S == 13) {
    z.badv |= cs_bad(z);
    z.csum = cs_sum(z);
    asm volatile("" : "+v"(z.csum), "+v"(z.badv));
  }
}
constexpr bool has_gap_work(int S) { return wrap_in(S) >= 0 || S == 4 || S == 12 || S == 13; }
template <int N> DI void waitv() { asm volatile("s_waitcnt vmcnt(%0)" ::"i"(N) : "memory"); }

template <int TYPE, int S, int Q> DI void dma_slot(Ctx& k, const Late& z) {
  if constexpr (issued(S - 1) + Q < issued(S)) piece<issued(S - 1) + Q, TYPE>(k, z);
}

template <int TYPE> DI void first_reads(Late& z) {
  if (TYPE == 0) {
    z.csv = *(LAS float*)(z.pex);
#pragma unroll
    for (int i = 3; i < 5; ++i) { z.rs[i] = *(LAS f32x4*)(z.prs + i * 64); z.nm[i] = *(LAS f32x4*)(z.prs + (L_NM - L_RS) + i * 64); }
  } else { z.st[0] = *(LAS f32x4*)(z.pst); z.st[2] = *(LAS f32x4*)(z.pex); z.cs[0] = *(LAS f32x4*)(z.pc); z.cs[1] = *(LAS f32x4*)(z.pc + 64); }
}

template <int TYPE, int S> DI void kstep(Ctx& k, Acc& a, Late& z, const Bases& B, u32 lane, u32& stamp) {
  Frag f;
  constexpr u32 wa = WS5[S % 5] << 11, wi = wa / 59392, wo = wa % 59392, xi = (S >> 1) % 3, sub = S & 1;
  if (S == 13) {
    if (TYPE == 0)
      asm volatile("s_cmp_lg_u32 %5, 0\n\ts_cbranch_scc1 1f\n\tds_write_b128 %0, %1\n\tds_write_b32 %2, %3\n\tds_write_b32 %2, %4 offset:%6\n1:" ::"v"(z.pg), "v"(z.tv), "v"(z.pp),
                   "v"(z.tv[3]), "v"(z.tv[0]), "s"(k.t), "i"(L_NM - L_RS) : "memory", "scc");
    else asm volatile("s_cmp_lg_u32 %2, 0\n\ts_cbranch_scc1 1f\n\tds_write_b128 %0, %1\n1:" ::"v"(z.pg), "v"(z.tv), "s"(k.t) : "memory", "scc");
  }
  if (S == 14)
    asm volatile("s_cmp_lg_u32 %6, 0\n\ts_cbranch_scc1 1f\n\tds_write_b32 %0, %1\n\tds_write_b32 %2, %3\n\tds_or_b32 %4, %5\n1:" ::"v"(z.pcw), "v"(z.csum[0]), "v"(z.pc4),
                 "v"(z.csum[1]), "v"(z.pb), "v"(z.badv), "s"(k.t) : "memory", "scc");
  LAS char* px = B.x[xi][sub];
  LAS char* px2 = B.y[xi][sub];
  auto xaddr = [&](int i) { return TYPE == 0 ? px + i * 2048 : (i < 2 ? px : px2) + (i & 1) * 4096; };
  LAS char* pa = B.wa[wi] + wo;
  f.w[0] = ldtr(pa); f.x[0] = ld128(px); f.x[1] = ld128(xaddr(1));
  dma_slot<TYPE, S, 0>(k, z);
  f.x[2] = ld128(xaddr(2)); f.x[3] = ld128(xaddr(3)); f.w[1] = ldtr(pa + 4096);
  dma_slot<TYPE, S, 1>(k, z);
  f.w[2] = ldtr(pa + 8192); f.w[3] = ldtr(pa + 12288);
  dma_slot<TYPE, S, 2>(k, z);
  if (TYPE == 0) { f.w[4] = ldtr(pa + 16384); f.hx = ld128(B.y[xi][sub]); }
  else { f.w[4] = ldtr(B.wc[wi] + wo); f.w[5] = ldtr(B.wd[wi] + wo); }
  dma_slot<TYPE, S, 3>(k, z);
  SB();
  if constexpr (issued(S) < 56) { asm volatile("" : "+s"(k.so0), "+s"(k.so1), "+s"(k.sox)); asm volatile("" : "+v"(k.p2)); }
  if (S == 5)
    asm volatile("s_cmp_lg_u32 %4, 0\n\ts_cbranch_scc1 1f\n\tbuffer_store_dwordx4 %0, %1, %2, %3 offen sc1\n\ts_nop 1\n1:" ::"v"(z.gr), "v"(z.hl), "s"(z.rsw),
                 "s"(z.sc + (k.panel & 3) * 4096), "s"(k.t) : "memory", "scc");
  if (S == TCH) {
    asm volatile("s_mov_b32 m0, %1\n\ts_nop 0\n\tglobal_load_lds_dword %0, off" ::"v"(z.ta), "i"(L_RS) : "memory");
    asm volatile("s_cmp_gt_u32 %4, 2\n\ts_cbranch_scc1 1f\n\ts_add_u32 m0, %3, %5\n\ts_nop 0\n\tbuffer_load_dwordx4 %0, %1, %2 offen sc1 lds\n1:" ::"v"(z.hl), "s"(z.rsw),
                 "s"(WS_STATS - 256 + k.panel * 2048 + k.w * 1024), "s"(k.d3), "s"(k.w), "i"(L_GRAN) : "memory", "scc");
  }
  if (S == CSL)
    asm volatile("s_cmp_lg_u32 %10, 0\n\ts_cbranch_scc1 1f\n\t" CS_LOADS "1:" : "=&v"(z.cp[0]), "=&v"(z.cp[1]), "=&v"(z.cp[2]), "=&v"(z.cp[3])
                 : "v"(z.hl), "s"(z.rsw), "s"(z.sc), "s"(z.sc + 4096), "s"(z.sc + 8192), "s"(z.sc + 12288), "s"(k.t) : "memory", "scc");
  if (S == T3S)
    asm volatile("s_mov_b32 m0, %3\n\ts_nop 0\n\tbuffer_load_dword %0, %1, %2 offen lds" ::"v"(z.v3t), "s"(z.rs2), "s"(k.d3), "i"(L_GRAN + 144 * 16) : "memory");
  if (S == 12) z.gr = ld128(z.pg);
  if (S == 15) { first_reads<TYPE>(z); z.badv = *(LAS u32*)z.pb; }
  if (S == 0)
    asm volatile("s_mov_b64 exec, 1\n\tglobal_store_dwordx4 %0, %1, %2 sc1\n\ts_mov_b64 exec, -1\n\ts_nop 0" ::"v"(z.vst), "v"(z.gr), "s"(z.ws) : "memory");
  if constexpr (S < 15) {
    static_assert(issued(S) >= needed(S), "schedule");
    waitv<issued(S) - needed(S) + (needed(S) <= issued(0)) + (S >= TCH && needed(S) <= issued(TCH))>();
    static_assert(needed(13) > issued(CSL) && needed(11) > issued(TCH), "hand-off loads are covered");
    if (S == 13) asm volatile("" : "+v"(z.cp[0]), "+v"(z.cp[1]), "+v"(z.cp[2]), "+v"(z.cp[3]));
  }
  BAR();
  if (has_gap_work(S)) gap_work<TYPE, S>(k, a, z);
#pragma unroll
  for (int n = 0; n < 22 - 2 * TYPE; ++n) {
    if (n < 20) main_mfma<TYPE>(a, f, n);
    else if (n == 20) a.e[0] = mfma16(f.w[1], f.hx, a.e[0]);
    else a.e[1] = mfma16(f.hx, f.w[2], a.e[1]);
  }
  if (has_gap_work(S)) {
#pragma unroll
    for (int n = 0; n < 22 - 2 * TYPE; ++n) { __builtin_amdgcn_sched_group_barrier(0x8, 1, 0); __builtin_amdgcn_sched_group_barrier(0x6, 1, 0); }
  }
  SB();
#pragma unroll
  for (int j = 0; j < 5; ++j) asm volatile("" : "+v"(a.m[j][0]), "+v"(a.m[j][1]), "+v"(a.m[j][2]), "+v"(a.m[j][3]));
  if (TYPE == 0) asm volatile("" : "+v"(a.e[0]), "+v"(a.e[1]));
  if (S < 4) {
#define CSM(i, j) "v_mfma_f32_16x16x32_bf16 %" #i ", %6, %" #j ", %" #i "\n\t"
    if (TYPE == 0)
      asm volatile("s_cmp_lg_u32 %5, 0\n\ts_cbranch_scc1 1f\n\t" CSM(0, 7) CSM(1, 8) CSM(2, 9) CSM(3, 10) CSM(4, 11) "\n1:"
                   : "+v"(a.cs[0]), "+v"(a.cs[1]), "+v"(a.cs[2]), "+v"(a.cs[3]), "+v"(a.cs[4])
                   : "s"(k.t), "v"(a.ones), "v"(f.w[0]), "v"(f.w[1]), "v"(f.w[2]), "v"(f.w[3]), "v"(f.w[4]) : "scc");
    else
      asm volatile("s_cmp_lg_u32 %13, 0\n\ts_cbranch_scc1 1f\n\t" CSM(0, 7) CSM(1, 8) CSM(2, 9) CSM(3, 10) CSM(4, 11) CSM(5, 12) "\n1:"
                   : "+v"(a.cs[0]), "+v"(a.cs[1]), "+v"(a.cs[2]), "+v"(a.cs[3]), "+v"(a.cs[4]), "+v"(a.cs[5])
                   : "v"(a.ones), "v"(f.w[0]), "v"(f.w[1]), "v"(f.w[2]), "v"(f.w[3]), "v"(f.w[4]), "v"(f.w[5]), "s"(k.t) : "scc");
  }
  SB();
  if (S < 15) asm volatile("s_barrier" ::: "memory");
  SB();
  if constexpr (S < 15) kstep<TYPE, S + 1>(k, a, z, B, lane, stamp);
}

template <int TYPE>
DI void kloop(Ctx& k, Acc& a, Late& z, const Bases& B, u32 lane, u32& stamp) {
#pragma unroll
  for (int j = 0; j < 5; ++j)
#pragma unroll
    for (int i = 0; i < 4; ++i) a.m[j][i] = f32x4{0.f, 0.f, 0.f, 0.f};
#pragma unroll
  for (int j = 0; j < 6; ++j) { a.e[j & 1] = f32x4{0.f, 0.f, 0.f, 0.f}; a.cs[j] = f32x4{0.f, 0.f, 0.f, 0.f}; }
  a.ones = u32x4{0x3F803F80u, 0x3F803F80u, 0x3F803F80u, 0x3F803F80u};
  asm volatile("" : "+v"(a.ones), "+v"(a.cs[0]), "+v"(a.cs[1]), "+v"(a.cs[2]), "+v"(a.cs[3]), "+v"(a.cs[4]));
  if (TYPE) asm volatile("" : "+v"(a.cs[5]));
  late_setup<TYPE>(k, z, lane);
  SB();
  waitv<PRE - 5>();
  STAMP(9);
  BAR();
  if (k.t) BAR();
  kstep<TYPE, 0>(k, a, z, B, lane, stamp);
  if (k.t == 0) asm volatile("s_barrier" ::: "memory");
  SB();
  if (__builtin_expect(__any(z.badv != 0), 0)) {
    if (k.t == 0) { stats_slow<TYPE>(k, z); *(LAS float*)z.pcw = z.csum[0]; *(LAS float*)z.pc4 = z.csum[1]; }
    BAR();
    first_reads<TYPE>(z);
  }
  z.q[2] = pin(z.q[0] + 65536); z.q[3] = pin(z.q[1] + 65536);
  if (TYPE == 0) {
    const u32 first = (k.panel + k.t) ? 0u : NEGB;
#pragma unroll
    for (int r = 0; r < 4; ++r) z.mA0[r] = __uint_as_float(__float_as_uint(z.mA[r]) | first);
  }
  if (TYPE == 0) {
#pragma unroll
    for (int i = 2; i >= 0; --i) { z.rs[i] = *(LAS f32x4*)(z.prs + i * 64); z.nm[i] = *(LAS f32x4*)(z.prs + (L_NM - L_RS) + i * 64); }
#pragma unroll
    for (int i = 0; i < 5; ++i) z.st[i] = *(LAS f32x4*)(z.pst + i * 256);
    z.cs[0] = *(LAS f32x4*)(z.pc); z.cs[1] = *(LAS f32x4*)(z.pc + 64);
  } else {
    z.st[1] = *(LAS f32x4*)(z.pst + 512); z.st[3] = *(LAS f32x4*)(z.pex + 512);
    z.cs[4] = *(LAS f32x4*)(z.prs); z.cs[5] = *(LAS f32x4*)(z.prs + 320); z.cs[2] = *(LAS f32x4*)(z.pc + 128); z.cs[3] = *(LAS f32x4*)(z.pc + 192);
  }
  SB();
}

DI u32x4 merge_tiles(u32x2 a, u32x2 b) {
  auto r0 = __builtin_amdgcn_permlane16_swap(a[0], b[0], false, false);
  auto r1 = __builtin_amdgcn_permlane16_swap(a[1], b[1], false, false);
  return u32x4{r0[0], r1[0], r0[1], r1[1]};
}
DI u32x2 glu_tile(f32x4 g, f32x4 u, f32x4 cg, f32x4 cu, f32x4 st) {
  const f32x4 gg = (g + cg * st[0]) * st[1], uu = u + cu * st[0];
  f32x4 e;
#pragma unroll
  for (int r = 0; r < 4; ++r) e[r] = __builtin_amdgcn_exp2f(gg[r]);
  const f32x4 den = e * st[2] + st[2];
#pragma unroll
  for (int r = 0; r < 4; ++r) e[r] = __builtin_amdgcn_rcpf(den[r]);
  const f32x4 h = uu * e;
  return u32x2{pk(h[0], h[1]), pk(h[2], h[3])};
}
DI u32x2 norm_tile(f32x4 v, f32x4 cs, float nm, float rs) {
  const f32x4 r = (v + cs * nm) * rs;
  return u32x2{pk(r[0], r[1]), pk(r[2], r[3])};
}
DI float max2(float a, float b) { return __builtin_elementwise_maximum(a, b); }
DI float max3(float a, float b, float c) { return max2(max2(a, b), c); }

struct Args {
  const char* tab[32][4];
  u32 tt[2][24];
  const char* w2[8];
  const char *x, *wo, *wd;
  char *out, *ws;
};

__global__ __launch_bounds__(512, 2) void fused_block(const Args A) {
  __shared__ __attribute__((aligned(1024))) char smem[163840];
  u32 stamp = 0;
  STAMP(0);
  u32 pf, tid = threadIdx.x, b = blockIdx.x;
  const u32 w = __builtin_amdgcn_readfirstlane(tid >> 6), t = w >> 2, c = w & 3;
  const u32 jb = b >> 3, type = b >> 7, idx = (b & 6) + ((b >> 6) & 1), slice = type * 8 + idx;
  const u32 panel = (b & 1) * 8 + (jb & 7);
  const char* const xg = A.x;
  char* const ws = A.ws;
  const u64 s1 = (u64)A.tab[slice * 2 + t][0], s2 = (u64)A.tab[slice * 2 + t][1], s0l = (u64)A.tab[slice * 2 + t][2], s0h = (u64)A.tab[slice * 2 + t][3];
  Ctx k;
  Late z;
  Bases B;
#pragma unroll
  for (int j = 0; j < 16; ++j) k.m2[j] = A.tt[t][j];
  k.inc2e = A.tt[t][16] | ((u64)A.tt[t][17] << 32); k.inc2o = A.tt[t][18] | ((u64)A.tt[t][19] << 32); k.wrap2 = A.tt[t][20];
  {
    u64 pc; u32 len;
    asm volatile("s_getpc_b64 %0\n.Lfb_begin:\n\ts_mov_b32 %1, .Lfb_end-.Lfb_begin" : "=s"(pc), "=s"(len));
    const u32 o = min((jb * 16 + (tid >> 5)) * 128u, len) & ~3u;
    asm volatile("global_load_dword %0, %1, %2" : "=v"(pf) : "v"(o), "s"(pc) : "memory");
  }
  u32 lane = tid & 63;
  u32 g = lane >> 4, n16 = lane & 15;
  const u32 srow = panel * 128 + slice * 8 + w;
  LAS char* const lds = (LAS char*)smem;
  const u32 lb = (u32)(size_t)lds;
  k.lds = lds; k.t = t; k.c = c; k.w = w; k.panel = panel; k.slice = slice; k.cb = P2_CB(b);
  k.rc = (panel + (jb >> 3)) & (ROT - 1);
  const u32 kk0 = k.rc * (512 / ROT);
  u32x2 ep; u32x4 r0;
  {
    const u32 rr = lane >> 3, pos = lane & 7;
    const u32 gcW = pos ^ (((g & 1) | ((c & 1) << 1)) << 1);
    const u32 row = 8 * c + rr;
    const u32 mul0 = type ? 3584 : 3072, mul1 = (type | t) ? 3584 : 3072;
    z.hl = lane * 16;
    k.vX = (panel * 128 + 8 * w + rr) * 1024 + (pos ^ (4 * (c & 1) + g)) * 16;
    k.v0 = row * mul0 + gcW * 16; k.v1 = row * mul1 + gcW * 16;
    const u32 ow = row * 3584 + gcW * 16;
    u32 o2 = (t ? k.vX : ow) + (t ? 32768 + kk0 * 2 : kk0 * 3584);
    u32 vr = srow * 1024 + z.hl;
    k.sox = kk0 * 2 + 16384; k.so0 = kk0 * mul0; k.so1 = kk0 * mul1; k.inc0 = 32 * mul0; k.inc1 = 32 * mul1;
    k.xh = panel ? 16384 : 0;
    k.onh = (3u >> (w | (type << 1))) & 1;
    k.d3 = lb + w * 1024; k.d2 = lb + c * 1024; k.d0 = k.d2 + t * 8192;
    asm volatile("" : "+v"(k.vX), "+v"(k.v0), "+v"(k.v1), "+v"(o2), "+v"(vr), "+v"(z.hl));
    asm volatile("" : "+s"(k.so0), "+s"(k.so1), "+s"(k.d0), "+s"(k.d2), "+s"(k.d3), "+s"(k.onh), "+s"(k.xh), "+s"(k.sox));
    SB();
    asm volatile("global_load_dwordx4 %0, %1, %2" : "=v"(r0) : "v"(vr), "s"(xg) : "memory");
    asm volatile("global_load_dwordx2 %0, %1, %2 sc1" : "=v"(ep) : "v"(0u), "s"(ws) : "memory");
    k.rsx = srd(xg - 16384);
    if (wraps(0)) set_wrap<0>(k);
    piece<0, 0>(k, z);
    k.rs0 = srd((const char*)s1); k.rs1 = srd((const char*)s2);
    piece<3, 0>(k, z); piece<4, 0>(k, z);
    k.p2 = (gcW < 4 ? s0l : s0h) + o2;
    piece<1, 0>(k, z); piece<2, 0>(k, z);
    asm volatile("s_barrier" ::: "memory");
    asm volatile("" : "+v"(lane));
    g = lane >> 4; n16 = lane & 15;
    SB();
    STAMP(8);
    const u32 q4 = n16 >> 2, f = (q4 >> 1) | ((g & 1) << 1);
    const u32 rowp = (8 * g + q4) * 128 + 8 * (lane & 3);
    const u32 rW = rowp + ((c ^ f) * 32), rW4 = rowp + (((c >> 1) ^ f) * 32) + 16384;
    const u32 xl = n16 * 128 + ((g ^ (n16 >> 1)) * 16);
    const u32 ox = t * 8192 + (type & c) * 2048, oy = type ? ox ^ 2048 : (t ? 6144 : 16384);
#pragma unroll
    for (int i = 0; i < 3; ++i) {
      B.wa[i] = pin(lds + (rW + i * 59392)); B.wc[i] = pin(lds + (rW4 + i * 59392)); B.wd[i] = pin(lds + ((rW4 ^ 64) + i * 59392));
#pragma unroll
      for (int o = 0; o < 2; ++o) {
        B.x[i][o] = pin(lds + ((xl ^ (o * 64)) + ((XS3[i] << 11) + ox)));
        B.y[i][o] = pin(lds + ((xl ^ (o * 64)) + ((XS3[i] << 11) + oy)));
      }
    }
    *(LAS u32*)(lds + L_CNT + lane * 4) = 0u;
  }
#define RH 1
  z.rho = RH == 0 ? 2 * ((2 * (jb & 1) + (jb >> 2)) & 3) : RH == 1 ? 2 * (((jb >> 2) + (k.cb == 3 ? jb : 0)) & 3) : RH == 2 ? 2 * (((jb >> 2) + (k.cb == 3 ? 2 * jb : 0)) & 3)
        : RH == 3 ? (k.cb == 3 ? 2 * (jb & 3) : 0) : 2 * (((jb >> 2) + (k.cb == 3 ? jb : 2 * jb)) & 3);
   z.w2 = (u64)A.w2[w];
  z.ws = ws; z.vst = WS_STATS + srow * 16;
  z.rsw = srd(ws); z.rsw[2] = WS_H + H_BYTES;
  {
    const u32 ln = (b * 8 + w) * 9 + min(lane, 8u);
    z.ta = (ln < 4096 ? (u64)A.wo : (u64)A.wd - 4096 * 128) + ln * 128;
    asm volatile("" : "+v"(z.ta), "+v"(z.vst));
    SB();
    piece<5, 0>(k, z); piece<6, 0>(k, z);
    asm volatile("s_waitcnt vmcnt(7)" : "+v"(ep), "+v"(r0), "+v"(pf)::"memory");
    z.tlo = __builtin_amdgcn_readfirstlane(ep[0]) + 1; z.thi = __builtin_amdgcn_readfirstlane(ep[1]) + 1;
    float v[8];
#pragma unroll
    for (int i = 0; i < 4; ++i) { v[2 * i] = lo(r0[i]); v[2 * i + 1] = hi(r0[i]); }
    const float mean = add32(sum32lanes(((v[0] + v[1]) + (v[2] + v[3])) + ((v[4] + v[5]) + (v[6] + v[7])))) * (1.f / 512.f);
    float qq = 0.f;
#pragma unroll
    for (int i = 0; i < 8; ++i) { const float d = v[i] - mean; qq += d * d; }
    const float var = rbf(add32(sum32lanes(qq)) * (1.f / 512.f));
    const float sd = rbf(__builtin_amdgcn_sqrtf(rbf(var + 1e-5f)));
    z.gr = u32x4{pk(mean, sd), z.tlo, __float_as_uint(__builtin_amdgcn_rcpf(sd)), z.thi};
    asm volatile("" : "+v"(z.gr));
    SB();
    if (PRE > 7 && wraps(1)) set_wrap<1>(k);
    if (PRE > 7) piece<7, 0>(k, z);
    if (PRE > 8) piece<8, 0>(k, z);
    if (PRE > 9) piece<9, 0>(k, z);
    if (PRE > 10) piece<10, 0>(k, z);
    if (PRE > 11) piece<11, 0>(k, z);
    if (PRE > 12) piece<12, 0>(k, z);
    if (PRE > 13) piece<13, 0>(k, z);
  }
  const u32 knum = (u32)((0x1312101312131310ULL >> (8 * w)) & 0xff);
  k.knum = knum; k.q4 = jb & 3;
  const u32 dw2 = k.d3, rho = z.rho;
  const __amdgpu_buffer_rsrc_t rsw = __builtin_amdgcn_make_buffer_rsrc((void*)ws, 0, WS_H + H_BYTES, 0x00020000);
  u32x4 w2f[19][2];
  auto rdw2 = [&](int dstj, int j) {
    w2f[dstj][0] = ldtr(z.q[(j >> 2) * 2] + (j & 3) * 16384); w2f[dstj][1] = ldtr(z.q[(j >> 2) * 2 + 1] + (j & 3) * 16384);
  };
#define W2A(p) do { SB(); dmaw<(p) * 8192>(z.vg[(p) >> 2], z.rs2, dw2); SB(); } while (0)
#define W2P(p) do { waitv<THR>(); W2A(p); } while (0)
#define FLIP(i) z.vg[i] += (((i + rho) & 4) ? (u32)-131072 : 131072u)
  const u32 b3 = lb + 131072 + (w - 1 - (w > 5)) * 4096;
  const u32 rb = P2_RB(b), cb = P2_CB(b);
  const u64 tag = ((u64)z.thi << 32) | z.tlo;
  GAS u64* flags = (GAS u64*)(ws + WS_FLAGS);
#define FLIPS() do { FLIP(0); FLIP(1); FLIP(2); FLIP(3); } while (0)
#define HSV(v, vo, o) asm volatile("buffer_store_dwordx4 %0, %1, %2, %3 offen sc1\n\ts_nop 1" ::"v"(v), "v"(vo), "s"(z.rsw), "s"(o) : "memory")
#define STAMP2(i) STAMP(i)
  auto signal = [&](u32 tile) {
    u32 old;
    asm volatile("s_mov_b64 exec, 1\n\tds_add_rtn_u32 %0, %1, %2\n\ts_waitcnt lgkmcnt(0)\n\tv_cmp_eq_u32 vcc, 3, %0\n\ts_nop 4\n\ts_cbranch_vccz 1f\n\t"
                 "buffer_store_dwordx2 %3, off, %4, %5 sc1\n1:\n\ts_mov_b64 exec, -1"
                 : "=&v"(old) : "v"(lb + L_CNT + t * 8 + tile * 4), "v"(1u), "v"(tag), "s"(z.rsw), "s"(WS_FLAGS + (((panel * 2 + t) * 2 + tile) * 16 + slice) * 8)
                 : "memory", "vcc");
  };
  static_assert(NW2 == 12, "pieces left for the epilogue");
  u32 type2 = type;
  if (type != 0) {
    Acc a;
    kloop<1>(k, a, z, B, lane, stamp);
    STAMP(1);
    STAMP(2);
    __builtin_amdgcn_s_setprio(1);
    const u32 s0 = 32 + 32 + 10 * idx;
    const u32 h0 = z.hbase + (s0 + (idx & 1) * 2 + c) * 1024, hm = z.hbase + (s0 + ((idx & 1) ? 0 : 8) + (c >> 1)) * 1024;
    u32x2 u, v;
    W2A(12); W2A(13);
    u = glu_tile(a.m[0][0], a.m[1][0], z.cs[0], z.cs[1], z.st[0]);
    W2A(14); W2A(15);
    v = glu_tile(a.m[0][2], a.m[1][2], z.cs[0], z.cs[1], z.st[2]);
    HSV(merge_tiles(u, v), z.hs, h0);
    SB(); WAITV(11);
    rdw2(0, 0); rdw2(1, 1); rdw2(2, 2);
    FLIPS();
    u = glu_tile(a.m[2][0], a.m[3][0], z.cs[2], z.cs[3], z.st[0]);
    SB(); WAITL();
    W2A(0);
    v = glu_tile(a.m[2][2], a.m[3][2], z.cs[2], z.cs[3], z.st[2]);
    HSV(merge_tiles(u, v), z.hs, h0 + 4096);
    W2A(1);
    u = glu_tile(a.m[4][0], a.m[4][2], z.cs[4], z.cs[5], z.st[0]);
    v = glu_tile(a.m[4][1], a.m[4][3], z.cs[4], z.cs[5], z.st[1]);
    HSV(merge_tiles(u, v), z.hm, hm);
    __builtin_amdgcn_s_setprio(0);
    SB(); WAITV(9);
    rdw2(3, 3); rdw2(4, 4); rdw2(5, 5);
    W2A(2); W2A(3);
    u = glu_tile(a.m[0][1], a.m[1][1], z.cs[0], z.cs[1], z.st[1]);
    W2A(4); W2A(5);
    v = glu_tile(a.m[0][3], a.m[1][3], z.cs[0], z.cs[1], z.st[3]);
    const u32x4 m0 = merge_tiles(u, v);
    u = glu_tile(a.m[2][1], a.m[3][1], z.cs[2], z.cs[3], z.st[1]);
    asm volatile("" : "+v"(u));
    SB(); WAITV(4);
    signal(0);
    STAMP2(3);
    HSV(m0, z.hs, h0 + 144 * 1024);
    rdw2(6, 6); rdw2(7, 7);
    W2A(6);
    v = glu_tile(a.m[2][3], a.m[3][3], z.cs[2], z.cs[3], z.st[3]);
    HSV(merge_tiles(u, v), z.hs, h0 + 4096 + 144 * 1024);
    SB();
#pragma unroll
    for (int i = 0; i < 3; ++i)
      asm volatile("" : "=v"(B.wa[i]), "=v"(B.x[i][0]), "=v"(B.x[i][1]), "=v"(B.y[i][0]), "=v"(B.y[i][1]));
    asm volatile("" : "=v"(k.v0), "=v"(k.v1), "=v"(k.vX), "=v"(k.p2), "=v"(z.gr), "=v"(z.vst), "=v"(z.ta));
#pragma unroll
    for (int i = 0; i < 16; ++i) asm volatile("" : "=s"(k.m2[i]));
    asm volatile("" : "=s"(k.rs0), "=s"(k.rs1), "=s"(k.rsx), "=s"(k.inc2e), "=s"(k.inc2o), "=s"(k.w2o), "=s"(z.w2));
  }
  asm volatile("" : "+s"(type2));
  if (type2 == 0) {
    Acc a;
    kloop<0>(k, a, z, B, lane, stamp);
    STAMP(1);
    STAMP(2);
    __builtin_amdgcn_s_setprio(1);
    const f32x4 csg = *(LAS f32x4*)(z.pc + 192), csu = *(LAS f32x4*)(z.pc + 256);
    const u32 oo = z.hbase + (4 * idx + c) * 1024;
    const u32x4 ones = {0x3F803F80u, 0x3F803F80u, 0x3F803F80u, 0x3F803F80u};
    const f32x4 zz = {0.f, 0.f, 0.f, 0.f};
    u32x2 kf[5], vf[5], hh[2], of[2];
    float l0[2], l1[2], mx[2], m1[2], m2[2], m3[2];
    f32x4 sum[2], ov[2];
    auto logits = [&](int i0) {
#pragma unroll
      for (int j = 0; j < 2; ++j) {
        const int i = i0 + j;
        kf[i + 1] = norm_tile(a.m[1][i], z.cs[1], z.st[i + 1][0], z.st[i + 1][3]);
        const f32x4 q = a.m[0][i] + z.cs[0] * z.st[i + 1][0];
        const u32x2 qf = {pk(q[0], q[1]), pk(q[2], q[3])};
        const f32x4 sa = mfma16k16(kf[i], qf, i ? z.mA : z.mA0), sb = mfma16k16(kf[i + 1], qf, z.mB);
        l0[j] = max2(max3(sa[0], sa[1], sb[0]), sb[1]); l1[j] = max2(max3(sa[2], sa[3], sb[2]), sb[3]);
        mx[j] = max2(l0[j], l1[j]);
        m1[j] = __int_as_float(__builtin_amdgcn_ds_swizzle(__float_as_int(mx[j]), 0x401F));
        m2[j] = __int_as_float(__builtin_amdgcn_ds_bpermute((z.hl >> 2) ^ 128, __float_as_int(mx[j])));
        m3[j] = __int_as_float(__builtin_amdgcn_ds_bpermute((z.hl >> 2) ^ 192, __float_as_int(mx[j])));
      }
    };
    auto pv = [&](int i0) {
#pragma unroll
      for (int j = 0; j < 2; ++j) {
        const int i = i0 + j;
        const float m = max2(max3(mx[j], m1[j], m2[j]), m3[j]), sc = z.st[i + 1][1] * -0.25f;
        const u32 pp = pk(__builtin_amdgcn_exp2f((l0[j] - m) * sc), __builtin_amdgcn_exp2f((l1[j] - m) * sc));
        const u32x4 pf4 = {__builtin_amdgcn_perm(pp, pp, z.sel[0]), __builtin_amdgcn_perm(pp, pp, z.sel[1]), __builtin_amdgcn_perm(pp, pp, z.sel[2]),
                           __builtin_amdgcn_perm(pp, pp, z.sel[3])};
        const u32x4 vv = {vf[i][0], vf[i][1], vf[i + 1][0], vf[i + 1][1]};
        sum[j] = mfma16(ones, pf4, zz);
        ov[j] = mfma16(vv, pf4, zz);
      }
    };
    auto outp = [&] {
#pragma unroll
      for (int j = 0; j < 2; ++j) { const f32x4 o = ov[j] * __builtin_amdgcn_rcpf(sum[j][0]); of[j] = u32x2{pk(o[0], o[1]), pk(o[2], o[3])}; }
    };
    W2A(12); W2A(13);
#pragma unroll
    for (int i = 4; i >= 0; --i) {
      const f32x4 v = ((i ? a.m[2][i ? i - 1 : 0] : a.e[1]) + z.nm[i] * z.csv) * z.rs[i];
      vf[i] = u32x2{pk(v[0], v[1]), pk(v[2], v[3])};
    }
    W2A(14); W2A(15);
    kf[0] = norm_tile(a.e[0], z.cs[1], z.st[0][0], z.st[0][3]);
    logits(0);
    SB(); WAITV(10);
    rdw2(0, 0); rdw2(1, 1); rdw2(2, 2);
    FLIPS();
    hh[0] = glu_tile(a.m[3][0], a.m[4][0], csg, csu, z.st[1]);
    SB(); WAITL();
    W2A(0);
    pv(0);
    W2A(1);
    hh[1] = glu_tile(a.m[3][1], a.m[4][1], csg, csu, z.st[2]);
    outp();
    HSV(merge_tiles(hh[0], hh[1]), z.hl, oo + 32 * 1024);
    HSV(merge_tiles(of[0], of[1]), z.hl, oo);
    __builtin_amdgcn_s_setprio(0);
    SB(); WAITV(8);
    rdw2(3, 3); rdw2(4, 4); rdw2(5, 5);
    W2A(2); W2A(3);
    logits(2);
    W2A(4); W2A(5);
    hh[0] = glu_tile(a.m[3][2], a.m[4][2], csg, csu, z.st[3]);
    asm volatile("" : "+v"(hh[0]));
    SB(); WAITV(4);
    signal(0);
    STAMP2(3);
    rdw2(6, 6); rdw2(7, 7);
    W2A(6);
    pv(2);
    SB();
    hh[1] = glu_tile(a.m[3][3], a.m[4][3], csg, csu, z.st[4]);
    outp();
    HSV(merge_tiles(hh[0], hh[1]), z.hl, oo + (144 + 32) * 1024);
    HSV(merge_tiles(of[0], of[1]), z.hl, oo + 144 * 1024);
    SB();
  }
  asm volatile("" : "+v"(lane));
  g = lane >> 4; n16 = lane & 15;
  const u32 kbeg = (u32)((0x7D6B105846332000ULL >> (8 * w)) & 0xff), hl = z.hl;
  WAITL();
  W2A(7); W2A(8); W2A(9); W2A(10);
  WAITV(4);
  signal(1);
  STAMP2(4);
  W2A(11); W2A(12); W2A(13); W2A(14); W2A(15);
  if (knum > 16) {
    const u32 v3 = (z.vg[0] & 0x7fff) + (8 + 3) * 32768;
    dma<0>(v3, z.rs2, dw2, b3); dma<1024>(v3, z.rs2, dw2 + 8192, b3); dma<2048>(v3, z.rs2, dw2 + 16384, b3); dma<3072>(v3, z.rs2, dw2 + 24576, b3);
  }
  SB();
  auto wait_flags = [&](u32 tile) {
    volatile LAS u32* rdy = (volatile LAS u32*)(lds + L_CNT + 16 + tile * 4);
#pragma unroll 1
    for (u32 spin = 0; spin < SPIN_CAP; ++spin) {
      if (__builtin_amdgcn_readfirstlane(*rdy)) break;
      if (c == 0) {
        const u64 v = __hip_atomic_load(flags + (rb * 2 + tile) * 16 + n16, __ATOMIC_RELAXED, __HIP_MEMORY_SCOPE_AGENT);
        if (__all(v == tag)) { *rdy = 1; break; }
      }
      __builtin_amdgcn_s_sleep(PSLEEP);
    }
  };
  auto wait_own = [&](u32 grp) {
#pragma unroll 1
    for (u32 spin = 0; spin < SPIN_CAP; ++spin) {
      __builtin_amdgcn_s_sleep(4);
      const u64 v = __hip_atomic_load(flags + grp * 16 + n16, __ATOMIC_RELAXED, __HIP_MEMORY_SCOPE_AGENT);
      if (__all(v == tag)) break;
    }
  };
  wait_flags(0);
  __builtin_amdgcn_fence(__ATOMIC_ACQUIRE, "wavefront");
  SB();
  STAMP2(5);
  {
    const u32 hsrc = WS_H + (rb * 2 * 144 + kbeg) * 1024;
    u32x4 hf[WIN];
    auto hload = [&](int n) -> u32x4 { const int tt = n / 19, st = n % 19;
      const u32 o = hsrc + tt * (144 * 1024) + (st < 16 ? (((st >> 1) + rho) & 7) * 2048 : (st & ~1) * 1024);
      return __builtin_amdgcn_raw_buffer_load_b128(rsw, hl + (st & 1) * 1024, st < 16 ? o : ((u32)st < knum ? o : 0x40000000u), 16); };
#pragma unroll
    for (int n = 0; n < WIN; ++n) hf[n] = hload(n);
    SB();
    const u32 ooff = (rb * 64 + 4 * w + g) * 1024 + cb * 128 + n16 * 8;
    const u32 wpos = w * 1024 + ((lane & 48) | ((lane ^ (lane >> 5)) & 15)) * 16;
    const u32 tok = 4 * w + g;
    const u32 rpos = (n16 >> 1) * 8192 + (((n16 & 1) * 32) | (tok & 16) | ((tok ^ n16) & 15)) * 16;
    u32x2 xres[2];
    u64 f1 = 0;
    f32x4 p[8];
    auto fetch = [&](int tt) {
      LAS char* pr = lds + (rpos + (1 - tt) * 65536);
#pragma unroll
      for (int ww = 0; ww < 8; ++ww) p[ww] = *(LAS f32x4*)(pr + ww * 1024);
    };
    auto finish = [&](int tt) {
      const f32x4 s = p[0] + p[5], d = ((p[1] + p[2]) + (p[3] + p[4])) + (p[6] + p[7]);
      const f32x4 dl = d * -1.4426950408889634f, sd = s * d;
      f32x4 e;
#pragma unroll
      for (int i = 0; i < 4; ++i) e[i] = __builtin_amdgcn_exp2f(dl[i]);
      e += 1.0f;
#pragma unroll
      for (int i = 0; i < 4; ++i) e[i] = __builtin_amdgcn_rcpf(e[i]);
      const f32x4 xr = {lo(xres[tt][0]), hi(xres[tt][0]), lo(xres[tt][1]), hi(xres[tt][1])};
      const f32x4 r = sd * e + xr;
      *(GAS u32x2*)(A.out + ooff + tt * 32768) = u32x2{pk(r[0], r[1]), pk(r[2], r[3])};
    };
    f32x16 acc[2][2];
    auto flush = [&](int tt, int u) {
      const f32x16& s = acc[tt][u >> 2];
      const f32x4 v = {s[4 * (u & 3)], s[4 * (u & 3) + 1], s[4 * (u & 3) + 2], s[4 * (u & 3) + 3]};
      *(LAS f32x4*)(lds + ((wpos ^ (u * 32)) + (1 - tt) * 65536) + u * 8192) = v;
    };
#pragma unroll
    for (int n = 0; n < 38; ++n) {
      const int st = n % 19, tt = n / 19;
      if (st == 0) {
#pragma unroll
        for (int i = 0; i < 16; ++i) { acc[tt][0][i] = 0.f; acc[tt][1][i] = 0.f; }
      }
      if (st < 16 || (u32)st < knum) {
        acc[tt][0] = mfma32(w2f[st][0], hf[n % WIN], acc[tt][0]);
        if (n == 19) { SB(); flush(0, 0); flush(0, 1); flush(0, 2); flush(0, 3); SB(); }
        acc[tt][1] = mfma32(w2f[st][1], hf[n % WIN], acc[tt][1]);
      }
      SB();
      if (n + WIN == 19) {
        if (__builtin_expect(!__all(f1 == tag), 0)) wait_own(rb * 2 + 1);
        __builtin_amdgcn_fence(__ATOMIC_ACQUIRE, "wavefront");
        SB();
      }
      if (n + WIN < 38) hf[n % WIN] = hload(n + WIN);
      if (n == 1 && knum > 18) {
        const u32 v3 = (z.vg[0] & 0x7fff) + (8 + 3) * 32768 + 32768;
        WAITL(); dmaw<0>(v3, z.rs2, dw2); dmaw<8192>(v3, z.rs2, dw2);
      }
      if (n < 8) rdw2(8 + n, n);
      if (n == 8 || n == 9) {
        const u32 o3 = b3 - lb + (((u32)(size_t)z.q[0] - lb) & 1023) + (lane >> 5) * 1024;
        w2f[8 + n][0] = ldtr(lds + o3 + (n - 8) * 2048); w2f[8 + n][1] = ldtr(lds + (o3 ^ 64) + (n - 8) * 2048);
      }
      if (n == 13) rdw2(18, 0);
      if (n == FLN) f1 = __hip_atomic_load(flags + (rb * 2 + 1) * 16 + n16, __ATOMIC_RELAXED, __HIP_MEMORY_SCOPE_AGENT);
      if (n == 14) {
        xres[0] = *(const GAS u32x2*)(xg + ooff);
        xres[1] = *(const GAS u32x2*)(xg + ooff + 32768);
      }
      if (n >= 19 && n < 23) flush(0, n - 15);
      if (n == 30 && b == 0 && w == 0) {
#pragma unroll 1
        for (u32 spin = 0; spin < SPIN_CAP; ++spin) {
          bool ok = true;
#pragma unroll
          for (int i = 0; i < 8; ++i) ok &= __hip_atomic_load(flags + (i * 4 + g) * 32 + n16, __ATOMIC_RELAXED, __HIP_MEMORY_SCOPE_AGENT) == tag;
          if (__all(ok)) break;
          __builtin_amdgcn_s_sleep(1);
        }
        if (lane == 0) __hip_atomic_store((GAS u64*)(ws + WS_EPOCH), tag, __ATOMIC_RELAXED, __HIP_MEMORY_SCOPE_AGENT);
      }
      if (n == 18) { STAMP2(10); }
      if (n == 33) { STAMP2(11); }
      SB();
    }
#pragma unroll
    for (int u = 0; u < 8; ++u) flush(1, u);
    STAMP2(6);
    BAR();
    fetch(0);
    finish(0);
    fetch(1);
    finish(1);
  }
  asm volatile(".Lfb_end:");
  STAMP(7);
  if (b == 40 && w == 1) {
    const u32 s0 = __builtin_amdgcn_readlane(stamp, 0), sx = __builtin_amdgcn_readlane(stamp, SCI);
    const u32 tgt = 4000 + 8 * (sx - s0);
    while ((u32)__builtin_amdgcn_s_memrealtime() - s0 < tgt && tgt < 40000) __builtin_amdgcn_s_sleep(4);
  }
  if (lane < 48) ((u64*)(ws + WS_STAMP))[(lane >> 4) * 32768 + (b * 8 + w) * 16 + (lane & 15)] = stamp;
}

extern "C" void kernel_launch(void* const* d_in, const int* in_sizes, int n_in, void* d_out, int out_size, void* d_ws, size_t ws_size,
                              hipStream_t stream) {
  const char *x = (const char*)d_in[0], *wqkv = (const char*)d_in[1], *wg = (const char*)d_in[4], *wu = (const char*)d_in[5];
  Args A;
  for (int i = 0; i < 8; ++i) {
    const char** a0 = A.tab[2 * i]; const char** a1 = A.tab[2 * i + 1]; const char** b0 = A.tab[16 + 2 * i]; const char** b1 = A.tab[17 + 2 * i];
    const int f0 = 1024 + 320 * i, co = f0 + (i & 1) * 64, om = (i & 1) ? f0 : f0 + 256;
    a0[0] = wqkv + i * 128;        a0[1] = wqkv + i * 128 + 1024; a0[2] = wu + i * 128; a0[3] = wu + i * 128;
    a1[0] = wqkv + i * 128 + 2048; a1[1] = wg + i * 128;          a1[2] = x;            a1[3] = x;
    b0[0] = wg + co;               b0[1] = wu + co;               b0[2] = wg + om;      b0[3] = wu + om - 64;
    b1[0] = wg + co + 128;         b1[1] = wu + co + 128;         b1[2] = x;            b1[3] = x;
    const int kb = (int)((0x7D6B105846332000ULL >> (8 * i)) & 0xff);
    A.w2[i] = kb < 32 ? (const char*)d_in[2] + kb * 16384 : (const char*)d_in[6] + (kb - 32) * 16384;
  }
  for (int j = 0; j < 16; ++j) { A.tt[0][j] = (WS5[j % 5] << 11) + 16384; A.tt[1][j] = (XS3[(j >> 1) % 3] << 11) + 8192 + (j & 1) * 4096; }
  A.tt[0][16] = 32 * 3584; A.tt[0][17] = 0; A.tt[0][18] = 32 * 3584; A.tt[0][19] = 0; A.tt[0][20] = 512 * 3584;
  A.tt[1][16] = 32768; A.tt[1][17] = 0; A.tt[1][18] = (u32)(128 - 32768); A.tt[1][19] = 0xffffffffu; A.tt[1][20] = 1024;
  for (int j = 21; j < 24; ++j) { A.tt[0][j] = 0; A.tt[1][j] = 0; }
  A.x = x; A.wo = (const char*)d_in[2]; A.wd = (const char*)d_in[6]; A.out = (char*)d_out; A.ws = (char*)d_ws;
  fused_block<<<256, 512, 0, stream>>>(A);
}
```

```cpp
#include <hip/hip_runtime.h>

typedef unsigned short u16;
typedef unsigned u32;
typedef unsigned long long u64;
typedef short s16x4 __attribute__((ext_vector_type(4)));
typedef __bf16 bf16x8 __attribute__((ext_vector_type(8)));
typedef __bf16 bf16x2 __attribute__((ext_vector_type(2)));
typedef float f32x2 __attribute__((ext_vector_type(2)));
typedef float f32x4 __attribute__((ext_vector_type(4)));
typedef float f32x16 __attribute__((ext_vector_type(16)));
typedef u32 u32x2 __attribute__((ext_vector_type(2)));
typedef u32 u32x4 __attribute__((ext_vector_type(4)));
#define LAS __attribute__((address_space(3)))
#define GAS __attribute__((address_space(1)))
#define DI __device__ __forceinline__
#define SB() __builtin_amdgcn_sched_barrier(0)

#define P2_RB(b) (((b) & 7) * 4 + (((b) >> 3) & 3))
#define P2_CB(b) ((0x76534210u >> (((b) >> 5) * 4)) & 7)

constexpr u32 WS_EPOCH = 0;
constexpr u32 WS_FLAGS = 49152;
constexpr u32 WS_STATS = 8192;
constexpr u32 WS_CS = 65536;
constexpr u32 WS_H = 2097152;
constexpr u32 H_BYTES = 64u * 144u * 1024u;
constexpr u32 L_CS = 154 * 1024;
constexpr u32 L_CNT = L_CS + 1280;
constexpr u32 L_BAD = L_CNT + 128;
constexpr u32 L_GRAN = L_BAD + 128;
constexpr u32 L_RS = L_GRAN + 3072;
constexpr u32 L_NM = L_RS + 592;
static_assert(L_NM + 592 <= 163840, "LDS map");
constexpr u32 SPIN_CAP = 1u << 21;
constexpr int ROT = 4;
constexpr int PRE = 9;
constexpr int W2PER = 3;
constexpr int TCH = 8, CSL = 10, T3S = 15;
constexpr int WIN = 6, FLN = 8;
constexpr u32 WS5[5] = {58, 0, 19, 29, 48};
constexpr u32 XS3[3] = {39, 68, 10};

DI f32x4 mfma16(u32x4 a, u32x4 b, f32x4 c) {
  return __builtin_amdgcn_mfma_f32_16x16x32_bf16(__builtin_bit_cast(bf16x8, a), __builtin_bit_cast(bf16x8, b), c, 0, 0, 0);
}
DI f32x4 mfma16k16(u32x2 a, u32x2 b, f32x4 c) {
  return __builtin_amdgcn_mfma_f32_16x16x16bf16_1k(__builtin_bit_cast(s16x4, a), __builtin_bit_cast(s16x4, b), c, 0, 0, 0);
}
DI f32x16 mfma32(u32x4 a, u32x4 b, f32x16 c) {
  return __builtin_amdgcn_mfma_f32_32x32x16_bf16(__builtin_bit_cast(bf16x8, a), __builtin_bit_cast(bf16x8, b), c, 0, 0, 0);
}
DI u32 pk(float a, float b) { f32x2 v = {a, b}; return __builtin_bit_cast(u32, __builtin_convertvector(v, bf16x2)); }
DI float rbf(float a) { return __uint_as_float(pk(a, a) << 16); }
DI float lo(u32 v) { return __uint_as_float(v << 16); }
DI float hi(u32 v) { return __uint_as_float(v & 0xffff0000u); }
DI u32x4 srd(const char* p) { const u64 a = (u64)p; return u32x4{(u32)a, (u32)(a >> 32) & 0xffffu, 0x7fffffffu, 0x00020000u}; }

template <int IMM> DI void dma(u32 voff, u32x4 rs, u32 soff, u32 dst) {
  asm volatile("s_add_u32 m0, %3, %4\n\ts_nop 0\n\tbuffer_load_dwordx4 %0, %1, %2 offen sc1 lds" ::"v"(voff), "s"(rs), "s"(soff), "s"(dst), "i"(IMM)
               : "memory", "scc");
}
template <int IMM> DI void dmaw(u32 voff, u32x4 rs, u32 dst) {
  asm volatile("s_add_u32 m0, %2, %3\n\ts_nop 0\n\tbuffer_load_dwordx4 %0, %1, m0 offen sc1 lds" ::"v"(voff), "s"(rs), "s"(dst), "i"(IMM) : "memory", "scc");
}
template <int IMM> DI void dma_if(u32 on, u32 voff, u32x4 rs, u32 soff, u32 dst) {
  asm volatile("s_cmp_eq_u32 %5, 0\n\ts_cbranch_scc1 1f\n\ts_add_u32 m0, %3, %4\n\ts_nop 0\n\tbuffer_load_dwordx4 %0, %1, %2 offen sc1 lds\n1:" ::"v"(voff),
               "s"(rs), "s"(soff), "s"(dst), "i"(IMM), "s"(on)
               : "memory", "scc");
}
DI void dma64(const char* vaddr, u32 dst, u32 dst2) {
  asm volatile("s_add_u32 m0, %1, %2\n\ts_nop 0\n\tglobal_load_lds_dwordx4 %0, off sc1" ::"v"(vaddr), "s"(dst), "s"(dst2) : "memory", "scc");
}
#define WAITV(n) asm volatile("s_waitcnt vmcnt(" #n ")" ::: "memory")
#define WAITL() do { __builtin_amdgcn_s_waitcnt(0xC07F); asm volatile("" ::: "memory"); } while (0)
#define BAR() do { SB(); __builtin_amdgcn_s_waitcnt(0xC07F); asm volatile("s_barrier" ::: "memory"); SB(); } while (0)

DI u32x4 ld128(LAS char* p) { return *(LAS u32x4*)p; }
DI u32x4 ldtr(LAS char* p) {
  s16x4 a = __builtin_amdgcn_ds_read_tr16_b64_v4i16((LAS s16x4*)p);
  s16x4 b = __builtin_amdgcn_ds_read_tr16_b64_v4i16((LAS s16x4*)(p + 512));
  u32x2 x = __builtin_bit_cast(u32x2, a), y = __builtin_bit_cast(u32x2, b);
  return u32x4{x[0], x[1], y[0], y[1]};
}
template <int CTRL> DI float dpp(float v) {
  return __int_as_float(__builtin_amdgcn_update_dpp(0, __float_as_int(v), CTRL, 0xf, 0xf, true));
}
DI float add16(float v) { auto r = __builtin_amdgcn_permlane16_swap(__float_as_uint(v), __float_as_uint(v), false, false);
  return __uint_as_float(r[0]) + __uint_as_float(r[1]); }
DI float add32(float v) { auto r = __builtin_amdgcn_permlane32_swap(__float_as_uint(v), __float_as_uint(v), false, false);
  return __uint_as_float(r[0]) + __uint_as_float(r[1]); }
DI float sum32lanes(float v) {
  v += dpp<0xB1>(v);
  v += dpp<0x4E>(v);
  v += dpp<0x141>(v);
  v += dpp<0x140>(v);
  return add16(v);
}


struct Frag { u32x4 x[4]; u32x4 w[6]; u32x4 hx; };
struct Acc { f32x4 m[5][4]; f32x4 e[2]; f32x4 cs[6]; u32x4 ones; };

struct Ctx {
  LAS char* lds;
  u32x4 rs0, rs1, rsx;
  u64 p2;
  u32 v0, v1, vX;
  u32 so0, so1, sox;
  u32 inc0, inc1, rc;
  u64 inc2e, inc2o, w2o;
  u32 w0, w1, wx;
  u32 wrap2, onh, xh, t, c, w, panel, slice, cb, q4, knum;
  u32 d0, d2, d3;
  u32 m2[16];
};
struct Late {
  u32 vg[4], vst, hl, rho, hbase;
  u32x4 rsw, rs2, gr, sel;
  u32 tlo, thi;
  u32 v3t, need, badv, g1, g2, hs, hm;
  u64 w2;
  LAS char *pb, *pg, *pp, *pcw, *pc4, *pst, *pex, *prs, *pc, *q[4];
  const char* ws;
  f32x4 st[5], rs[5], nm[5], cs[6], mA, mA0, mB, tv;
  u32x4 cp[4];
  u32 sc;
  f32x2 csum;
  float csv;
  u64 ta;
};

constexpr int first_seg(int P) {
  if (P >= 56) { const int e = (P - 55) * 8; return e <= 38 ? 12 : e <= 58 ? 13 : e <= 96 ? 14 : e <= 116 ? 15 : 16; }
  const int m = P / 7, r = P % 7, j = 2 * m + (r == 2 || r >= 5);
  return r == 0 ? 2 * m - 4 : j - 4;
}
constexpr int issued(int S) {
  int P = PRE;
  for (int s = 0; s <= S; ++s) for (int q = 0, v = 0; q < 4 && first_seg(P) <= s && v < W2PER; ++q) { v += P >= 56; ++P; }
  return P;
}
constexpr int needed(int S) { return 7 * ((S + 1) >> 1) + (((S + 1) & 1) ? 7 : 5); }
constexpr int NW2 = issued(15) - 56;
constexpr bool wraps(int m) { return m < 7 && (m + 1) % (8 / ROT) == 0; }
template <int M> DI void set_wrap(Ctx& k) {
  const bool wrap = k.rc == ROT - (M + 1) / (8 / ROT);
  k.w0 = wrap ? k.inc0 * (u32)-15 : k.inc0; k.w1 = wrap ? k.inc1 * (u32)-15 : k.inc1; k.wx = wrap ? (u32)-896 : 128u;
  k.w2o = k.inc2o - (wrap ? k.wrap2 : 0);
  asm volatile("" : "+s"(k.w0), "+s"(k.w1), "+s"(k.wx), "+s"(k.w2o));
}
constexpr int wrap_in(int S) {
  for (int m = 1; m < 7; ++m) if (wraps(m) && 7 * m >= issued(S) && 7 * m < issued(S + 1)) return m;
  return -1;
}
struct Bases { LAS char *wa[3], *wc[3], *wd[3], *x[3][2], *y[3][2]; };
static_assert(ROT == 4, "the column sums are handed over between four rotation classes");
template <int P, int TYPE> DI void piece(Ctx& k, const Late& z) {
  SB();
  if constexpr (P >= 56) dmaw<(P - 56) * 8192>(z.vg[(P - 56) >> 2], z.rs2, k.d3);
  else {
    constexpr int m = P / 7, r = P % 7, j = 2 * m + (r == 2 || r >= 5);
    constexpr u32 dW = WS5[j % 5] << 11, dX = XS3[m % 3] << 11;
    constexpr bool W = wraps(m);
    if (r == 0) {
      dma<dX>(k.vX, k.rsx, k.sox, k.d3);
      if (TYPE == 0) dma_if<dX + 16384>(k.onh, k.vX, k.rsx, k.sox - k.xh, k.d3);
      k.sox += W ? k.wx : 128;
    } else if (r < 3) { dma64((const char*)k.p2, k.m2[j], k.d2); k.p2 += (r == 2) ? (W ? k.w2o : k.inc2o) : k.inc2e; }
    else if (r & 1) { dma<dW>(k.v0, k.rs0, k.so0, k.d0); k.so0 += (W && r == 5) ? k.w0 : k.inc0; }
    else { dma<dW + 4096>(k.v1, k.rs1, k.so1, k.d0); k.so1 += (W && r == 6) ? k.w1 : k.inc1; }
  }
  SB();
}

template <int TYPE> DI void main_mfma(Acc& a, const Frag& f, int n) {
  const int j = n >> 2, i = n & 3;
  if (TYPE == 0) {
    if (j == 2) a.m[j][i] = mfma16(f.x[i], f.w[j], a.m[j][i]);
    else a.m[j][i] = mfma16(f.w[j], f.x[i], a.m[j][i]);
  } else {
    if (j < 4) a.m[j][i] = mfma16(f.w[j], f.x[i], a.m[j][i]);
    else a.m[4][i] = mfma16(f.w[4 + (i >> 1)], f.x[i & 1], a.m[4][i]);
  }
}

DI u32 gran_off(const Ctx& k, const Late& z) { return z.hl + min(k.c, 2u) * 1024; }
template <int TYPE> DI bool gran_need(const Ctx& k, u32 o) { return (o < 144 * 16) & ((o >= 256) | (TYPE == 0 && k.panel != 0)); }
DI f32x4 stats_value(u32 need, u32x4 gr) {
  const u32 ms = need & gr[0];
  const float rs = __uint_as_float(need & gr[2]);
  return f32x4{-lo(ms), rs * -1.4426950408889634f, hi(ms), rs};
}
template <int TYPE> DI void stats_write(const Late& z, f32x4 v) {
  *(LAS f32x4*)(z.pg) = v;
  if (TYPE == 0) { *(LAS float*)z.pp = v[3]; *(LAS float*)(z.pp + L_NM - L_RS) = v[0]; }
}
DI u32 cs_bad(const Late& z) {
  u32 d = 0;
#pragma unroll
  for (int i = 0; i < 4; ++i) d |= (z.cp[i][1] ^ z.tlo) | (z.cp[i][3] ^ z.thi);
  return d;
}
DI f32x2 cs_sum(const Late& z) {
  return f32x2{(__uint_as_float(z.cp[0][0]) + __uint_as_float(z.cp[1][0])) + (__uint_as_float(z.cp[2][0]) + __uint_as_float(z.cp[3][0])),
               (__uint_as_float(z.cp[0][2]) + __uint_as_float(z.cp[1][2])) + (__uint_as_float(z.cp[2][2]) + __uint_as_float(z.cp[3][2]))};
}
#define CS_LOADS "buffer_load_dwordx4 %0, %4, %5, %6 offen sc1\n\tbuffer_load_dwordx4 %1, %4, %5, %7 offen sc1\n\t" \
                 "buffer_load_dwordx4 %2, %4, %5, %8 offen sc1\n\tbuffer_load_dwordx4 %3, %4, %5, %9 offen sc1\n"
template <int TYPE> DI void stats_slow(const Ctx& k, Late& z) {
  const u32 o = gran_off(k, z);
  u32x4 gr;
#pragma unroll 1
  for (u32 spin = 0; spin < SPIN_CAP; ++spin) {
    asm volatile("buffer_load_dwordx4 %0, %1, %2, %3 offen sc1\n\ts_waitcnt vmcnt(0)" : "=&v"(gr) : "v"(o), "s"(z.rsw), "s"(WS_STATS - 256 + k.panel * 2048) : "memory");
    asm volatile(CS_LOADS "\ts_waitcnt vmcnt(0)" : "=&v"(z.cp[0]), "=&v"(z.cp[1]), "=&v"(z.cp[2]), "=&v"(z.cp[3])
                 : "v"(z.hl), "s"(z.rsw), "s"(z.sc), "s"(z.sc + 4096), "s"(z.sc + 32768), "s"(z.sc + 36864) : "memory");
    if (__all(((z.need & ((gr[1] ^ z.tlo) | (gr[3] ^ z.thi))) | cs_bad(z)) == 0)) break;
    __builtin_amdgcn_s_sleep(2);
  }
  stats_write<TYPE>(z, stats_value(z.need, gr)); z.csum = cs_sum(z);
}

DI LAS char* pin(LAS char* p) { asm volatile("" : "+v"(p)); return p; }
constexpr u32 NEGB = 0xFF61B1E6u;

template <int TYPE> DI void late_setup(const Ctx& k, Late& z, u32 lane) {
  asm volatile("" : "+v"(lane));
  const u32 g = lane >> 4, n16 = lane & 15;
  const u32 vW2 = (lane >> 3) * 1024 + ((lane & 7) ^ ((g & 1) << 2)) * 16;
#pragma unroll
  for (int i = 0; i < 4; ++i) z.vg[i] = vW2 + (((i + z.rho) & 7) + 3 - i) * 32768;
  asm volatile("" : "+v"(z.vg[0]), "+v"(z.vg[1]), "+v"(z.vg[2]), "+v"(z.vg[3]));
  const u32 r2 = (lane >> 5) * 8192 + (n16 >> 2) * 128 + (((n16 >> 3) & 1) * 64) + (g & 1) * 32 + (lane & 3) * 8 + k.w * 1024;
  z.q[0] = pin(k.lds + r2); z.q[1] = pin(k.lds + (r2 ^ 64));
  z.pst = pin(k.lds + (L_GRAN + (k.t * 64 + n16 + (TYPE ? 16 + (k.c & 1) * 16 : 0)) * 16));
  z.pex = pin(k.lds + (TYPE ? L_GRAN + (k.t * 64 + n16 + 32 - (k.c & 1) * 16) * 16 : L_CS + k.c * 320 + 128 + n16 * 4));
  const u32 o = gran_off(k, z);
  z.pg = pin(k.lds + (L_GRAN + o));
  if (TYPE == 0) z.pp = pin(k.lds + L_RS + min(o >> 2, 576u));
  z.pb = pin(k.lds + (L_BAD + (lane & 31) * 4));
  z.v3t = 3 * 32768 + min(256 + k.q4 * 12 + min(lane, 11u), k.knum * 16 - 1) * 1024;
  asm volatile("" : "+v"(z.v3t));
  z.need = gran_need<TYPE>(k, o) ? ~0u : 0u;
  z.pcw = pin(k.lds + (L_CS + k.c * 320 + lane * 4));
  z.pc4 = pin(k.lds + (L_CS + k.c * 320 + 256 + n16 * 4));
  z.pc = pin(k.lds + (L_CS + k.c * 320 + g * 16));
  z.prs = pin(k.lds + (TYPE ? L_CS + (k.c & 2) * 320 + 256 + g * 16 : L_RS + (k.t * 64 + g * 4) * 4));
  if (TYPE) { z.hs = z.hl ^ ((k.c & 1) * 256); z.hm = (g & 1) * (144 * 1024) + (g >> 1) * 512 + (k.c & 1) * 256 + n16 * 16; asm volatile("" : "+v"(z.hs), "+v"(z.hm)); }
  z.g1 = (g & 1) ? ~0u : 0u; z.g2 = (g & 2) ? ~0u : 0u;
  asm volatile("" : "+v"(z.need), "+v"(z.g1), "+v"(z.g2));
  z.rs2 = srd((const char*)(z.w2 + k.cb * 128 - k.d3 - 3 * 32768));
  z.hbase = WS_H + (k.panel * 4 + k.t * 2) * (144 * 1024);
  z.sc = WS_CS + (k.slice * 16 + (k.panel & 6u)) * 4096 + k.c * 1024;
  asm volatile("" : "+s"(z.rs2), "+s"(z.hbase), "+s"(z.sc));
  if (TYPE == 0) {
#pragma unroll
    for (int r = 0; r < 4; ++r) {
      const int d = (int)n16 - (int)(4 * g + r);
      const u32 odd = (u32)((d << 31) >> 31), ng = (u32)(d >> 31);
      z.mB[r] = __uint_as_float(NEGB & (odd | ng)); z.mA[r] = __uint_as_float(NEGB & (odd | ~ng));
    }
    const u32 sh = (n16 & 1) * 16, s0 = __builtin_amdgcn_alignbit(0x0c0c0100u, 0x0c0c0100u, sh), s1 = __builtin_amdgcn_alignbit(0x0c0c0302u, 0x0c0c0302u, sh);
    const int d = (int)(n16 & 14) - (int)(4 * g);
    const u32 n0 = (u32)(d >> 31), n1 = (u32)((d - 2) >> 31), zz = 0x0c0c0c0cu;
    z.sel = u32x4{(n0 & s0) | (~n0 & zz), (n1 & s1) | (~n1 & zz), (n0 & zz) | (~n0 & s0), (n1 & zz) | (~n1 & s1)};
    asm volatile("" : "+v"(z.mA), "+v"(z.mB), "+v"(z.sel));
  }
}
DI u32 bsel(u32 m, float a, float b) { return (m & __float_as_uint(a)) | (~m & __float_as_uint(b)); }
template <int TYPE, int S> DI void gap_work(Ctx& k, const Acc& a, Late& z) {
  if constexpr (wrap_in(S) >= 0) { set_wrap<wrap_in(S)>(k); }
  if (S == 4) {
    const u32 lo2 = bsel(z.g1, a.cs[1][0], a.cs[0][0]), hi2 = bsel(z.g1, a.cs[3][0], a.cs[2][0]);
    z.gr = u32x4{(z.g2 & hi2) | (~z.g2 & lo2), z.tlo, TYPE ? bsel(0u - (k.c & 1), a.cs[5][0], a.cs[4][0]) : __float_as_uint(a.cs[4][0]), z.thi};
    asm volatile("" : "+v"(z.gr));
  }
  if (S == 12) {
    z.badv = z.need & ((z.gr[1] ^ z.tlo) | (z.gr[3] ^ z.thi));
    z.tv = stats_value(z.need, z.gr);
    asm volatile("" : "+v"(z.tv), "+v"(z.badv));
  }
  if (S == 13) {
    z.badv |= cs_bad(z);
    z.csum = cs_sum(z);
    asm volatile("" : "+v"(z.csum), "+v"(z.badv));
  }
}
constexpr bool has_gap_work(int S) { return wrap_in(S) >= 0 || S == 4 || S == 12 || S == 13; }
template <int N> DI void waitv() { asm volatile("s_waitcnt vmcnt(%0)" ::"i"(N) : "memory"); }

template <int TYPE, int S, int Q> DI void dma_slot(Ctx& k, const Late& z) {
  if constexpr (issued(S - 1) + Q < issued(S)) piece<issued(S - 1) + Q, TYPE>(k, z);
}

template <int TYPE> DI void first_reads(Late& z) {
  if (TYPE == 0) {
    z.csv = *(LAS float*)(z.pex);
#pragma unroll
    for (int i = 3; i < 5; ++i) { z.rs[i] = *(LAS f32x4*)(z.prs + i * 64); z.nm[i] = *(LAS f32x4*)(z.prs + (L_NM - L_RS) + i * 64); }
  } else { z.st[0] = *(LAS f32x4*)(z.pst); z.st[2] = *(LAS f32x4*)(z.pex); z.cs[0] = *(LAS f32x4*)(z.pc); z.cs[1] = *(LAS f32x4*)(z.pc + 64); }
}

template <int TYPE, int S> DI void kstep(Ctx& k, Acc& a, Late& z, const Bases& B, u32 lane) {
  Frag f;
  constexpr u32 wa = WS5[S % 5] << 11, wi = wa / 59392, wo = wa % 59392, xi = (S >> 1) % 3, sub = S & 1;
  if (S == 13) {
    if (TYPE == 0)
      asm volatile("s_cmp_lg_u32 %5, 0\n\ts_cbranch_scc1 1f\n\tds_write_b128 %0, %1\n\tds_write_b32 %2, %3\n\tds_write_b32 %2, %4 offset:%6\n1:" ::"v"(z.pg), "v"(z.tv), "v"(z.pp),
                   "v"(z.tv[3]), "v"(z.tv[0]), "s"(k.t), "i"(L_NM - L_RS) : "memory", "scc");
    else asm volatile("s_cmp_lg_u32 %2, 0\n\ts_cbranch_scc1 1f\n\tds_write_b128 %0, %1\n1:" ::"v"(z.pg), "v"(z.tv), "s"(k.t) : "memory", "scc");
  }
  if (S == 14)
    asm volatile("s_cmp_lg_u32 %6, 0\n\ts_cbranch_scc1 1f\n\tds_write_b32 %0, %1\n\tds_write_b32 %2, %3\n\tds_or_b32 %4, %5\n1:" ::"v"(z.pcw), "v"(z.csum[0]), "v"(z.pc4),
                 "v"(z.csum[1]), "v"(z.pb), "v"(z.badv), "s"(k.t) : "memory", "scc");
  LAS char* px = B.x[xi][sub];
  LAS char* px2 = B.y[xi][sub];
  auto xaddr = [&](int i) { return TYPE == 0 ? px + i * 2048 : (i < 2 ? px : px2) + (i & 1) * 4096; };
  LAS char* pa = B.wa[wi] + wo;
  f.w[0] = ldtr(pa); f.x[0] = ld128(px); f.x[1] = ld128(xaddr(1));
  dma_slot<TYPE, S, 0>(k, z);
  f.x[2] = ld128(xaddr(2)); f.x[3] = ld128(xaddr(3)); f.w[1] = ldtr(pa + 4096);
  dma_slot<TYPE, S, 1>(k, z);
  f.w[2] = ldtr(pa + 8192); f.w[3] = ldtr(pa + 12288);
  dma_slot<TYPE, S, 2>(k, z);
  if (TYPE == 0) { f.w[4] = ldtr(pa + 16384); f.hx = ld128(B.y[xi][sub]); }
  else { f.w[4] = ldtr(B.wc[wi] + wo); f.w[5] = ldtr(B.wd[wi] + wo); }
  dma_slot<TYPE, S, 3>(k, z);
  SB();
  if constexpr (issued(S) < 56) { asm volatile("" : "+s"(k.so0), "+s"(k.so1), "+s"(k.sox)); asm volatile("" : "+v"(k.p2)); }
  if (S == 5)
    asm volatile("s_cmp_lg_u32 %4, 0\n\ts_cbranch_scc1 1f\n\tbuffer_store_dwordx4 %0, %1, %2, %3 offen sc1\n\ts_nop 1\n1:" ::"v"(z.gr), "v"(z.hl), "s"(z.rsw),
                 "s"(z.sc + (k.panel & ~6u) * 4096), "s"(k.t) : "memory", "scc");
  if (S == TCH) {
    asm volatile("s_mov_b32 m0, %1\n\ts_nop 0\n\tglobal_load_lds_dword %0, off" ::"v"(z.ta), "s"((u32)(size_t)(k.lds + L_RS)) : "memory");
    asm volatile("s_cmp_gt_u32 %4, 2\n\ts_cbranch_scc1 1f\n\ts_add_u32 m0, %3, %5\n\ts_nop 0\n\tbuffer_load_dwordx4 %0, %1, %2 offen sc1 lds\n1:" ::"v"(z.hl), "s"(z.rsw),
                 "s"(WS_STATS - 256 + k.panel * 2048 + k.w * 1024), "s"(k.d3), "s"(k.w), "i"(L_GRAN) : "memory", "scc");
  }
  if (S == CSL)
    asm volatile("s_cmp_lg_u32 %10, 0\n\ts_cbranch_scc1 1f\n\t" CS_LOADS "1:" : "=&v"(z.cp[0]), "=&v"(z.cp[1]), "=&v"(z.cp[2]), "=&v"(z.cp[3])
                 : "v"(z.hl), "s"(z.rsw), "s"(z.sc), "s"(z.sc + 4096), "s"(z.sc + 32768), "s"(z.sc + 36864), "s"(k.t) : "memory", "scc");
  if (S == T3S)
    asm volatile("s_mov_b32 m0, %3\n\ts_nop 0\n\tbuffer_load_dword %0, %1, %2 offen lds" ::"v"(z.v3t), "s"(z.rs2), "s"(k.d3), "s"((u32)(size_t)(k.lds + L_GRAN + 144 * 16)) : "memory");
  if (S == 12) z.gr = ld128(z.pg);
  if (S == 15) { first_reads<TYPE>(z); z.badv = *(LAS u32*)z.pb; }
  if (S == 0)
    asm volatile("s_mov_b64 exec, 1\n\tglobal_store_dwordx4 %0, %1, %2 sc1\n\ts_mov_b64 exec, -1\n\ts_nop 0" ::"v"(z.vst), "v"(z.gr), "s"(z.ws) : "memory");
  if constexpr (S < 15) {
    static_assert(issued(S) >= needed(S), "schedule");
    waitv<issued(S) - needed(S) + (needed(S) <= issued(0)) + (S >= TCH && needed(S) <= issued(TCH))>();
    static_assert(needed(13) > issued(CSL) && needed(11) > issued(TCH), "hand-off loads are covered");
    if (S == 13) asm volatile("" : "+v"(z.cp[0]), "+v"(z.cp[1]), "+v"(z.cp[2]), "+v"(z.cp[3]));
  }
  BAR();
  if (has_gap_work(S)) gap_work<TYPE, S>(k, a, z);
#pragma unroll
  for (int n = 0; n < 22 - 2 * TYPE; ++n) {
    if (n < 20) main_mfma<TYPE>(a, f, n);
    else if (n == 20) a.e[0] = mfma16(f.w[1], f.hx, a.e[0]);
    else a.e[1] = mfma16(f.hx, f.w[2], a.e[1]);
  }
  if (has_gap_work(S)) {
#pragma unroll
    for (int n = 0; n < 22 - 2 * TYPE; ++n) { __builtin_amdgcn_sched_group_barrier(0x8, 1, 0); __builtin_amdgcn_sched_group_barrier(0x6, 1, 0); }
  }
  SB();
#pragma unroll
  for (int j = 0; j < 5; ++j) asm volatile("" : "+v"(a.m[j][0]), "+v"(a.m[j][1]), "+v"(a.m[j][2]), "+v"(a.m[j][3]));
  if (TYPE == 0) asm volatile("" : "+v"(a.e[0]), "+v"(a.e[1]));
  if (S < 4) {
#define CSM(i, j) "v_mfma_f32_16x16x32_bf16 %" #i ", %6, %" #j ", %" #i "\n\t"
    if (TYPE == 0)
      asm volatile("s_cmp_lg_u32 %5, 0\n\ts_cbranch_scc1 1f\n\t" CSM(0, 7) CSM(1, 8) CSM(2, 9) CSM(3, 10) CSM(4, 11) "\n1:"
                   : "+v"(a.cs[0]), "+v"(a.cs[1]), "+v"(a.cs[2]), "+v"(a.cs[3]), "+v"(a.cs[4])
                   : "s"(k.t), "v"(a.ones), "v"(f.w[0]), "v"(f.w[1]), "v"(f.w[2]), "v"(f.w[3]), "v"(f.w[4]) : "scc");
    else
      asm volatile("s_cmp_lg_u32 %13, 0\n\ts_cbranch_scc1 1f\n\t" CSM(0, 7) CSM(1, 8) CSM(2, 9) CSM(3, 10) CSM(4, 11) CSM(5, 12) "\n1:"
                   : "+v"(a.cs[0]), "+v"(a.cs[1]), "+v"(a.cs[2]), "+v"(a.cs[3]), "+v"(a.cs[4]), "+v"(a.cs[5])
                   : "v"(a.ones), "v"(f.w[0]), "v"(f.w[1]), "v"(f.w[2]), "v"(f.w[3]), "v"(f.w[4]), "v"(f.w[5]), "s"(k.t) : "scc");
  }
  SB();
  if (S < 15) asm volatile("s_barrier" ::: "memory");
  SB();
  if constexpr (S < 15) kstep<TYPE, S + 1>(k, a, z, B, lane);
}

template <int TYPE>
DI void kloop(Ctx& k, Acc& a, Late& z, const Bases& B, u32 lane) {
#pragma unroll
  for (int j = 0; j < 5; ++j)
#pragma unroll
    for (int i = 0; i < 4; ++i) a.m[j][i] = f32x4{0.f, 0.f, 0.f, 0.f};
#pragma unroll
  for (int j = 0; j < 6; ++j) { a.e[j & 1] = f32x4{0.f, 0.f, 0.f, 0.f}; a.cs[j] = f32x4{0.f, 0.f, 0.f, 0.f}; }
  a.ones = u32x4{0x3F803F80u, 0x3F803F80u, 0x3F803F80u, 0x3F803F80u};
  asm volatile("" : "+v"(a.ones), "+v"(a.cs[0]), "+v"(a.cs[1]), "+v"(a.cs[2]), "+v"(a.cs[3]), "+v"(a.cs[4]));
  if (TYPE) asm volatile("" : "+v"(a.cs[5]));
  late_setup<TYPE>(k, z, lane);
  SB();
  waitv<PRE - 5>();
  BAR();
  if (k.t) BAR();
  kstep<TYPE, 0>(k, a, z, B, lane);
  if (k.t == 0) asm volatile("s_barrier" ::: "memory");
  SB();
  if (__builtin_expect(__any(z.badv != 0), 0)) {
    if (k.t == 0) { stats_slow<TYPE>(k, z); *(LAS float*)z.pcw = z.csum[0]; *(LAS float*)z.pc4 = z.csum[1]; }
    BAR();
    first_reads<TYPE>(z);
  }
  z.q[2] = pin(z.q[0] + 65536); z.q[3] = pin(z.q[1] + 65536);
  if (TYPE == 0) {
    const u32 first = (k.panel + k.t) ? 0u : NEGB;
#pragma unroll
    for (int r = 0; r < 4; ++r) z.mA0[r] = __uint_as_float(__float_as_uint(z.mA[r]) | first);
  }
  if (TYPE == 0) {
#pragma unroll
    for (int i = 2; i >= 0; --i) { z.rs[i] = *(LAS f32x4*)(z.prs + i * 64); z.nm[i] = *(LAS f32x4*)(z.prs + (L_NM - L_RS) + i * 64); }
#pragma unroll
    for (int i = 0; i < 5; ++i) z.st[i] = *(LAS f32x4*)(z.pst + i * 256);
    z.cs[0] = *(LAS f32x4*)(z.pc); z.cs[1] = *(LAS f32x4*)(z.pc + 64);
  } else {
    z.st[1] = *(LAS f32x4*)(z.pst + 512); z.st[3] = *(LAS f32x4*)(z.pex + 512);
    z.cs[4] = *(LAS f32x4*)(z.prs); z.cs[5] = *(LAS f32x4*)(z.prs + 320); z.cs[2] = *(LAS f32x4*)(z.pc + 128); z.cs[3] = *(LAS f32x4*)(z.pc + 192);
  }
  SB();
}

DI u32x4 merge_tiles(u32x2 a, u32x2 b) {
  auto r0 = __builtin_amdgcn_permlane16_swap(a[0], b[0], false, false);
  auto r1 = __builtin_amdgcn_permlane16_swap(a[1], b[1], false, false);
  return u32x4{r0[0], r1[0], r0[1], r1[1]};
}
DI u32x2 glu_tile(f32x4 g, f32x4 u, f32x4 cg, f32x4 cu, f32x4 st) {
  const f32x4 gg = (g + cg * st[0]) * st[1], uu = u + cu * st[0];
  f32x4 e;
#pragma unroll
  for (int r = 0; r < 4; ++r) e[r] = __builtin_amdgcn_exp2f(gg[r]);
  const f32x4 den = e * st[2] + st[2];
#pragma unroll
  for (int r = 0; r < 4; ++r) e[r] = __builtin_amdgcn_rcpf(den[r]);
  const f32x4 h = uu * e;
  return u32x2{pk(h[0], h[1]), pk(h[2], h[3])};
}
DI u32x2 norm_tile(f32x4 v, f32x4 cs, float nm, float rs) {
  const f32x4 r = (v + cs * nm) * rs;
  return u32x2{pk(r[0], r[1]), pk(r[2], r[3])};
}
DI float max2(float a, float b) { return __builtin_elementwise_maximum(a, b); }
DI float max3(float a, float b, float c) { return max2(max2(a, b), c); }

struct Args {
  const char* tab[32][4];
  u32 tt[2][24];
  const char* w2[8];
  const char *x, *wo, *wd;
  char *out, *ws;
};

__global__ __launch_bounds__(512, 2) void fused_block(const Args A) {
  __shared__ __attribute__((aligned(1024))) char smem[163840];
  u32 pf, tid = threadIdx.x, b = blockIdx.x;
  const u32 w = __builtin_amdgcn_readfirstlane(tid >> 6), t = w >> 2, c = w & 3;
  const u32 jb = b >> 3, type = b >> 7, idx = (b & 6) + ((b >> 6) & 1), slice = type * 8 + idx;
  const u32 panel = (b & 1) * 8 + (jb & 7);
  const char* const xg = A.x;
  char* const ws = A.ws;
  const u64 s1 = (u64)A.tab[slice * 2 + t][0], s2 = (u64)A.tab[slice * 2 + t][1], s0l = (u64)A.tab[slice * 2 + t][2], s0h = (u64)A.tab[slice * 2 + t][3];
  Ctx k;
  Late z;
  Bases B;
#pragma unroll
  for (int j = 0; j < 16; ++j) k.m2[j] = A.tt[t][j];
  k.inc2e = A.tt[t][16] | ((u64)A.tt[t][17] << 32); k.inc2o = A.tt[t][18] | ((u64)A.tt[t][19] << 32); k.wrap2 = A.tt[t][20];
  {
    u64 pc; u32 len;
    asm volatile("s_getpc_b64 %0\n.Lfb_begin:\n\ts_mov_b32 %1, .Lfb_end-.Lfb_begin" : "=s"(pc), "=s"(len));
    const u32 o = min((jb * 16 + (tid >> 5)) * 128u, len) & ~3u;
    asm volatile("global_load_dword %0, %1, %2" : "=v"(pf) : "v"(o), "s"(pc) : "memory");
  }
  u32 lane = tid & 63;
  u32 g = lane >> 4, n16 = lane & 15;
  const u32 srow = panel * 128 + slice * 8 + w;
  LAS char* const lds = (LAS char*)smem;
  const u32 lb = (u32)(size_t)lds;
  k.lds = lds; k.t = t; k.c = c; k.w = w; k.panel = panel; k.slice = slice; k.cb = P2_CB(b);
  k.rc = 2 * ((panel + (jb >> 3)) & 1) + (panel >> 3);
  const u32 kk0 = k.rc * (512 / ROT);
  u32x2 ep; u32x4 r0;
  {
    const u32 rr = lane >> 3, pos = lane & 7;
    const u32 gcW = pos ^ (((g & 1) | ((c & 1) << 1)) << 1);
    const u32 row = 8 * c + rr;
    const u32 mul0 = type ? 3584 : 3072, mul1 = (type | t) ? 3584 : 3072;
    z.hl = lane * 16;
    k.vX = (panel * 128 + 8 * w + rr) * 1024 + (pos ^ (4 * (c & 1) + g)) * 16;
    k.v0 = row * mul0 + gcW * 16; k.v1 = row * mul1 + gcW * 16;
    const u32 ow = row * 3584 + gcW * 16;
    u32 o2 = (t ? k.vX : ow) + (t ? 32768 + kk0 * 2 : kk0 * 3584);
    u32 vr = srow * 1024 + z.hl;
    k.sox = kk0 * 2 + 16384; k.so0 = kk0 * mul0; k.so1 = kk0 * mul1; k.inc0 = 32 * mul0; k.inc1 = 32 * mul1;
    k.xh = panel ? 16384 : 0;
    k.onh = (3u >> (w | (type << 1))) & 1;
    k.d3 = lb + w * 1024; k.d2 = lb + c * 1024; k.d0 = k.d2 + t * 8192;
    asm volatile("" : "+v"(k.vX), "+v"(k.v0), "+v"(k.v1), "+v"(o2), "+v"(vr), "+v"(z.hl));
    asm volatile("" : "+s"(k.so0), "+s"(k.so1), "+s"(k.d0), "+s"(k.d2), "+s"(k.d3), "+s"(k.onh), "+s"(k.xh), "+s"(k.sox));
    SB();
    asm volatile("global_load_dwordx4 %0, %1, %2" : "=v"(r0) : "v"(vr), "s"(xg) : "memory");
    asm volatile("global_load_dwordx2 %0, %1, %2 sc1" : "=v"(ep) : "v"(0u), "s"(ws) : "memory");
    k.rsx = srd(xg - 16384);
    piece<0, 0>(k, z);
    k.rs0 = srd((const char*)s1); k.rs1 = srd((const char*)s2);
    piece<3, 0>(k, z); piece<4, 0>(k, z);
    k.p2 = (gcW < 4 ? s0l : s0h) + o2;
    piece<1, 0>(k, z); piece<2, 0>(k, z);
    asm volatile("s_barrier" ::: "memory");
    asm volatile("" : "+v"(lane));
    g = lane >> 4; n16 = lane & 15;
    SB();
    const u32 q4 = n16 >> 2, f = (q4 >> 1) | ((g & 1) << 1);
    const u32 rowp = (8 * g + q4) * 128 + 8 * (lane & 3);
    const u32 rW = rowp + ((c ^ f) * 32), rW4 = rowp + (((c >> 1) ^ f) * 32) + 16384;
    const u32 xl = n16 * 128 + ((g ^ (n16 >> 1)) * 16);
    const u32 ox = t * 8192 + (type & c) * 2048, oy = type ? ox ^ 2048 : (t ? 6144 : 16384);
#pragma unroll
    for (int i = 0; i < 3; ++i) {
      B.wa[i] = pin(lds + (rW + i * 59392)); B.wc[i] = pin(lds + (rW4 + i * 59392)); B.wd[i] = pin(lds + ((rW4 ^ 64) + i * 59392));
#pragma unroll
      for (int o = 0; o < 2; ++o) {
        B.x[i][o] = pin(lds + ((xl ^ (o * 64)) + ((XS3[i] << 11) + ox)));
        B.y[i][o] = pin(lds + ((xl ^ (o * 64)) + ((XS3[i] << 11) + oy)));
      }
    }
    *(LAS u32*)(lds + L_CNT + lane * 4) = 0u;
  }
  z.rho = k.cb == 3 ? 2 * (jb & 3) : 0;
  z.w2 = (u64)A.w2[w];
  z.ws = ws; z.vst = WS_STATS + srow * 16;
  z.rsw = srd(ws); z.rsw[2] = WS_H + H_BYTES;
  {
    const u32 ln = (b * 8 + w) * 9 + min(lane, 8u);
    z.ta = (ln < 4096 ? (u64)A.wo : (u64)A.wd - 4096 * 128) + ln * 128;
    asm volatile("" : "+v"(z.ta), "+v"(z.vst));
    SB();
    piece<5, 0>(k, z); piece<6, 0>(k, z);
    asm volatile("s_waitcnt vmcnt(7)" : "+v"(ep), "+v"(r0), "+v"(pf)::"memory");
    z.tlo = __builtin_amdgcn_readfirstlane(ep[0]) + 1; z.thi = __builtin_amdgcn_readfirstlane(ep[1]) + 1;
    float v[8];
#pragma unroll
    for (int i = 0; i < 4; ++i) { v[2 * i] = lo(r0[i]); v[2 * i + 1] = hi(r0[i]); }
    const float mean = add32(sum32lanes(((v[0] + v[1]) + (v[2] + v[3])) + ((v[4] + v[5]) + (v[6] + v[7])))) * (1.f / 512.f);
    float qq = 0.f;
#pragma unroll
    for (int i = 0; i < 8; ++i) { const float d = v[i] - mean; qq += d * d; }
    const float var = rbf(add32(sum32lanes(qq)) * (1.f / 512.f));
    const float sd = rbf(__builtin_amdgcn_sqrtf(rbf(var + 1e-5f)));
    z.gr = u32x4{pk(mean, sd), z.tlo, __float_as_uint(__builtin_amdgcn_rcpf(sd)), z.thi};
    asm volatile("" : "+v"(z.gr));
    SB();
    set_wrap<1>(k);
    piece<7, 0>(k, z); piece<8, 0>(k, z);
    static_assert(PRE == 9 && wraps(1) && !wraps(0), "prologue");
  }
  const u32 knum = (u32)((0x1312101312131310ULL >> (8 * w)) & 0xff);
  k.knum = knum; k.q4 = jb & 3;
  const u32 dw2 = k.d3, rho = z.rho;
  const __amdgpu_buffer_rsrc_t rsw = __builtin_amdgcn_make_buffer_rsrc((void*)ws, 0, WS_H + H_BYTES, 0x00020000);
  u32x4 w2f[19][2];
  auto rdw2 = [&](int dstj, int j) {
    w2f[dstj][0] = ldtr(z.q[(j >> 2) * 2] + (j & 3) * 16384); w2f[dstj][1] = ldtr(z.q[(j >> 2) * 2 + 1] + (j & 3) * 16384);
  };
#define W2A(p) do { SB(); dmaw<(p) * 8192>(z.vg[(p) >> 2], z.rs2, dw2); SB(); } while (0)
#define FLIP(i) z.vg[i] += (((i + rho) & 4) ? (u32)-131072 : 131072u)
  const u32 b3 = lb + 131072 + (w - 1 - (w > 5)) * 4096;
  const u32 rb = P2_RB(b), cb = P2_CB(b);
  const u64 tag = ((u64)z.thi << 32) | z.tlo;
  GAS u64* flags = (GAS u64*)(ws + WS_FLAGS);
#define FLIPS() do { FLIP(0); FLIP(1); FLIP(2); FLIP(3); } while (0)
#define HSV(v, vo, o) asm volatile("buffer_store_dwordx4 %0, %1, %2, %3 offen sc1\n\ts_nop 1" ::"v"(v), "v"(vo), "s"(z.rsw), "s"(o) : "memory")
  auto signal = [&](u32 tile) {
    u32 old;
    asm volatile("s_mov_b64 exec, 1\n\tds_add_rtn_u32 %0, %1, %2\n\ts_waitcnt lgkmcnt(0)\n\tv_cmp_eq_u32 vcc, 3, %0\n\ts_nop 4\n\ts_cbranch_vccz 1f\n\t"
                 "buffer_store_dwordx2 %3, off, %4, %5 sc1\n1:\n\ts_mov_b64 exec, -1"
                 : "=&v"(old) : "v"(lb + L_CNT + t * 8 + tile * 4), "v"(1u), "v"(tag), "s"(z.rsw), "s"(WS_FLAGS + (((panel * 2 + t) * 2 + tile) * 16 + slice) * 8)
                 : "memory", "vcc");
  };
  static_assert(NW2 == 12, "pieces left for the epilogue");
  u32 type2 = type;
  if (type != 0) {
    Acc a;
    kloop<1>(k, a, z, B, lane);
    __builtin_amdgcn_s_setprio(1);
    const u32 s0 = 32 + 32 + 10 * idx;
    const u32 h0 = z.hbase + (s0 + (idx & 1) * 2 + c) * 1024, hm = z.hbase + (s0 + ((idx & 1) ? 0 : 8) + (c >> 1)) * 1024;
    u32x2 u, v;
    W2A(12); W2A(13);
    u = glu_tile(a.m[0][0], a.m[1][0], z.cs[0], z.cs[1], z.st[0]);
    W2A(14); W2A(15);
    v = glu_tile(a.m[0][2], a.m[1][2], z.cs[0], z.cs[1], z.st[2]);
    u32x4 k0 = merge_tiles(u, v), k1;
    SB(); WAITV(10);
    rdw2(0, 0); rdw2(1, 1); rdw2(2, 2);
    FLIPS();
    u = glu_tile(a.m[2][0], a.m[3][0], z.cs[2], z.cs[3], z.st[0]);
    SB(); WAITL();
    W2A(0);
    v = glu_tile(a.m[2][2], a.m[3][2], z.cs[2], z.cs[3], z.st[2]);
    k1 = merge_tiles(u, v);
    W2A(1);
    u = glu_tile(a.m[4][0], a.m[4][2], z.cs[4], z.cs[5], z.st[0]);
    v = glu_tile(a.m[4][1], a.m[4][3], z.cs[4], z.cs[5], z.st[1]);
    { const u32x4 k2 = merge_tiles(u, v); HSV(k2, z.hm, hm); HSV(k0, z.hs, h0); HSV(k1, z.hs, h0 + 4096); }
    __builtin_amdgcn_s_setprio(0);
    SB(); WAITV(9);
    rdw2(3, 3); rdw2(4, 4); rdw2(5, 5);
    W2A(2); W2A(3);
    u = glu_tile(a.m[0][1], a.m[1][1], z.cs[0], z.cs[1], z.st[1]);
    W2A(4); W2A(5);
    v = glu_tile(a.m[0][3], a.m[1][3], z.cs[0], z.cs[1], z.st[3]);
    const u32x4 m0 = merge_tiles(u, v);
    u = glu_tile(a.m[2][1], a.m[3][1], z.cs[2], z.cs[3], z.st[1]);
    asm volatile("" : "+v"(u));
    SB(); WAITV(4);
    signal(0);
    rdw2(6, 6); rdw2(7, 7);
    W2A(6);
    v = glu_tile(a.m[2][3], a.m[3][3], z.cs[2], z.cs[3], z.st[3]);
    HSV(merge_tiles(u, v), z.hs, h0 + 4096 + 144 * 1024);
    HSV(m0, z.hs, h0 + 144 * 1024);
    SB();
#pragma unroll
    for (int i = 0; i < 3; ++i)
      asm volatile("" : "=v"(B.wa[i]), "=v"(B.x[i][0]), "=v"(B.x[i][1]), "=v"(B.y[i][0]), "=v"(B.y[i][1]));
    asm volatile("" : "=v"(k.v0), "=v"(k.v1), "=v"(k.vX), "=v"(k.p2), "=v"(z.gr), "=v"(z.vst), "=v"(z.ta));
#pragma unroll
    for (int i = 0; i < 16; ++i) asm volatile("" : "=s"(k.m2[i]));
    asm volatile("" : "=s"(k.rs0), "=s"(k.rs1), "=s"(k.rsx), "=s"(k.inc2e), "=s"(k.inc2o), "=s"(k.w2o), "=s"(z.w2));
  }
  asm volatile("" : "+s"(type2));
  if (type2 == 0) {
    Acc a;
    kloop<0>(k, a, z, B, lane);
    __builtin_amdgcn_s_setprio(1);
    const f32x4 csg = *(LAS f32x4*)(z.pc + 192), csu = *(LAS f32x4*)(z.pc + 256);
    const u32 oo = z.hbase + (4 * idx + c) * 1024;
    const u32x4 ones = {0x3F803F80u, 0x3F803F80u, 0x3F803F80u, 0x3F803F80u};
    const f32x4 zz = {0.f, 0.f, 0.f, 0.f};
    u32x2 kf[5], vf[5], hh[2], of[2];
    float l0[2], l1[2], mx[2], m1[2], m2[2], m3[2];
    f32x4 sum[2], ov[2];
    auto logits = [&](int i0) {
#pragma unroll
      for (int j = 0; j < 2; ++j) {
        const int i = i0 + j;
        kf[i + 1] = norm_tile(a.m[1][i], z.cs[1], z.st[i + 1][0], z.st[i + 1][3]);
        const f32x4 q = a.m[0][i] + z.cs[0] * z.st[i + 1][0];
        const u32x2 qf = {pk(q[0], q[1]), pk(q[2], q[3])};
        const f32x4 sa = mfma16k16(kf[i], qf, i ? z.mA : z.mA0), sb = mfma16k16(kf[i + 1], qf, z.mB);
        l0[j] = max2(max3(sa[0], sa[1], sb[0]), sb[1]); l1[j] = max2(max3(sa[2], sa[3], sb[2]), sb[3]);
        mx[j] = max2(l0[j], l1[j]);
        m1[j] = __int_as_float(__builtin_amdgcn_ds_swizzle(__float_as_int(mx[j]), 0x401F));
        m2[j] = __int_as_float(__builtin_amdgcn_ds_bpermute((z.hl >> 2) ^ 128, __float_as_int(mx[j])));
        m3[j] = __int_as_float(__builtin_amdgcn_ds_bpermute((z.hl >> 2) ^ 192, __float_as_int(mx[j])));
      }
    };
    auto pv = [&](int i0) {
#pragma unroll
      for (int j = 0; j < 2; ++j) {
        const int i = i0 + j;
        const float m = max2(max3(mx[j], m1[j], m2[j]), m3[j]), sc = z.st[i + 1][1] * -0.25f;
        const u32 pp = pk(__builtin_amdgcn_exp2f((l0[j] - m) * sc), __builtin_amdgcn_exp2f((l1[j] - m) * sc));
        const u32x4 pf4 = {__builtin_amdgcn_perm(pp, pp, z.sel[0]), __builtin_amdgcn_perm(pp, pp, z.sel[1]), __builtin_amdgcn_perm(pp, pp, z.sel[2]),
                           __builtin_amdgcn_perm(pp, pp, z.sel[3])};
        const u32x4 vv = {vf[i][0], vf[i][1], vf[i + 1][0], vf[i + 1][1]};
        sum[j] = mfma16(ones, pf4, zz);
        ov[j] = mfma16(vv, pf4, zz);
      }
    };
    auto outp = [&] {
#pragma unroll
      for (int j = 0; j < 2; ++j) { const f32x4 o = ov[j] * __builtin_amdgcn_rcpf(sum[j][0]); of[j] = u32x2{pk(o[0], o[1]), pk(o[2], o[3])}; }
    };
    W2A(12); W2A(13);
#pragma unroll
    for (int i = 4; i >= 0; --i) {
      const f32x4 v = ((i ? a.m[2][i ? i - 1 : 0] : a.e[1]) + z.nm[i] * z.csv) * z.rs[i];
      vf[i] = u32x2{pk(v[0], v[1]), pk(v[2], v[3])};
    }
    W2A(14); W2A(15);
    kf[0] = norm_tile(a.e[0], z.cs[1], z.st[0][0], z.st[0][3]);
    logits(0);
    SB(); WAITV(10);
    rdw2(0, 0); rdw2(1, 1); rdw2(2, 2);
    FLIPS();
    hh[0] = glu_tile(a.m[3][0], a.m[4][0], csg, csu, z.st[1]);
    SB(); WAITL();
    W2A(0);
    pv(0);
    W2A(1);
    hh[1] = glu_tile(a.m[3][1], a.m[4][1], csg, csu, z.st[2]);
    outp();
    HSV(merge_tiles(hh[0], hh[1]), z.hl, oo + 32 * 1024);
    HSV(merge_tiles(of[0], of[1]), z.hl, oo);
    __builtin_amdgcn_s_setprio(0);
    SB(); WAITV(8);
    rdw2(3, 3); rdw2(4, 4); rdw2(5, 5);
    W2A(2); W2A(3);
    logits(2);
    W2A(4); W2A(5);
    hh[0] = glu_tile(a.m[3][2], a.m[4][2], csg, csu, z.st[3]);
    asm volatile("" : "+v"(hh[0]));
    SB(); WAITV(4);
    signal(0);
    rdw2(6, 6); rdw2(7, 7);
    W2A(6);
    pv(2);
    SB();
    hh[1] = glu_tile(a.m[3][3], a.m[4][3], csg, csu, z.st[4]);
    outp();
    HSV(merge_tiles(hh[0], hh[1]), z.hl, oo + (144 + 32) * 1024);
    HSV(merge_tiles(of[0], of[1]), z.hl, oo + 144 * 1024);
    SB();
  }
  asm volatile("" : "+v"(lane));
  g = lane >> 4; n16 = lane & 15;
  const u32 kbeg = (u32)((0x7D6B105846332000ULL >> (8 * w)) & 0xff), hl = z.hl;
  WAITL();
  W2A(7); W2A(8); W2A(9); W2A(10);
  WAITV(4);
  signal(1);
  W2A(11); W2A(12); W2A(13); W2A(14); W2A(15);
  if (knum > 16) {
    const u32 v3 = (z.vg[0] & 0x7fff) + (8 + 3) * 32768;
    dma<0>(v3, z.rs2, dw2, b3); dma<1024>(v3, z.rs2, dw2 + 8192, b3); dma<2048>(v3, z.rs2, dw2 + 16384, b3); dma<3072>(v3, z.rs2, dw2 + 24576, b3);
  }
  SB();
  auto wait_flags = [&](u32 tile) {
    volatile LAS u32* rdy = (volatile LAS u32*)(lds + L_CNT + 16 + tile * 4);
#pragma unroll 1
    for (u32 spin = 0; spin < SPIN_CAP; ++spin) {
      if (__builtin_amdgcn_readfirstlane(*rdy)) break;
      if (w == 0) {
        const u64 v = __hip_atomic_load(flags + (rb * 2 + tile) * 16 + n16, __ATOMIC_RELAXED, __HIP_MEMORY_SCOPE_AGENT);
        if (__all(v == tag)) { *rdy = 1; break; }
      }
      __builtin_amdgcn_s_sleep(4);
    }
  };
  auto wait_own = [&](u32 grp) {
#pragma unroll 1
    for (u32 spin = 0; spin < SPIN_CAP; ++spin) {
      __builtin_amdgcn_s_sleep(4);
      const u64 v = __hip_atomic_load(flags + grp * 16 + n16, __ATOMIC_RELAXED, __HIP_MEMORY_SCOPE_AGENT);
      if (__all(v == tag)) break;
    }
  };
  wait_flags(0);
  __builtin_amdgcn_fence(__ATOMIC_ACQUIRE, "wavefront");
  SB();
  if (t) __builtin_amdgcn_s_setprio(1);
  {
    const u32 hsrc = WS_H + (rb * 2 * 144 + kbeg) * 1024;
    u32x4 hf[WIN];
    auto hload = [&](int n) -> u32x4 { const int tt = n / 19, st = n % 19;
      const u32 o = hsrc + tt * (144 * 1024) + (st < 16 ? (((st >> 1) + rho) & 7) * 2048 : (st & ~1) * 1024);
      return __builtin_amdgcn_raw_buffer_load_b128(rsw, hl + (st & 1) * 1024, st < 16 ? o : ((u32)st < knum ? o : 0x40000000u), 16); };
#pragma unroll
    for (int n = 0; n < WIN; ++n) hf[n] = hload(n);
    SB();
    const u32 ooff = (rb * 64 + 4 * w + g) * 1024 + cb * 128 + n16 * 8;
    const u32 wpos = w * 1024 + ((lane & 48) | ((lane ^ (lane >> 5)) & 15)) * 16;
    const u32 tok = 4 * w + g;
    const u32 rpos = (n16 >> 1) * 8192 + (((n16 & 1) * 32) | (tok & 16) | ((tok ^ n16) & 15)) * 16;
    u32x2 xres[2];
    u64 f1 = 0;
    f32x4 p[8];
    auto fetch = [&](int tt) {
      LAS char* pr = lds + (rpos + (1 - tt) * 65536);
#pragma unroll
      for (int ww = 0; ww < 8; ++ww) p[ww] = *(LAS f32x4*)(pr + ww * 1024);
    };
    auto finish = [&](int tt) {
      const f32x4 s = p[0] + p[5], d = ((p[1] + p[2]) + (p[3] + p[4])) + (p[6] + p[7]);
      const f32x4 dl = d * -1.4426950408889634f, sd = s * d;
      f32x4 e;
#pragma unroll
      for (int i = 0; i < 4; ++i) e[i] = __builtin_amdgcn_exp2f(dl[i]);
      e += 1.0f;
#pragma unroll
      for (int i = 0; i < 4; ++i) e[i] = __builtin_amdgcn_rcpf(e[i]);
      const f32x4 xr = {lo(xres[tt][0]), hi(xres[tt][0]), lo(xres[tt][1]), hi(xres[tt][1])};
      const f32x4 r = sd * e + xr;
      *(GAS u32x2*)(A.out + ooff + tt * 32768) = u32x2{pk(r[0], r[1]), pk(r[2], r[3])};
    };
    f32x16 acc[2][2];
    auto flush = [&](int tt, int u) {
      const f32x16& s = acc[tt][u >> 2];
      const f32x4 v = {s[4 * (u & 3)], s[4 * (u & 3) + 1], s[4 * (u & 3) + 2], s[4 * (u & 3) + 3]};
      *(LAS f32x4*)(lds + ((wpos ^ (u * 32)) + (1 - tt) * 65536) + u * 8192) = v;
    };
#pragma unroll
    for (int n = 0; n < 38; ++n) {
      const int st = n % 19, tt = n / 19;
      if (st == 0) {
#pragma unroll
        for (int i = 0; i < 16; ++i) { acc[tt][0][i] = 0.f; acc[tt][1][i] = 0.f; }
      }
      if (st < 16 || (u32)st < knum) {
        acc[tt][0] = mfma32(w2f[st][0], hf[n % WIN], acc[tt][0]);
        if (n == 19) { SB(); flush(0, 0); flush(0, 1); flush(0, 2); flush(0, 3); SB(); }
        acc[tt][1] = mfma32(w2f[st][1], hf[n % WIN], acc[tt][1]);
      }
      SB();
      if (n + WIN == 19) {
        if (__builtin_expect(!__all(f1 == tag), 0)) wait_own(rb * 2 + 1);
        __builtin_amdgcn_fence(__ATOMIC_ACQUIRE, "wavefront");
        SB();
      }
      if (n + WIN < 38) hf[n % WIN] = hload(n + WIN);
      if (n == 1 && knum > 18) {
        const u32 v3 = (z.vg[0] & 0x7fff) + (8 + 3) * 32768 + 32768;
        WAITL(); dmaw<0>(v3, z.rs2, dw2); dmaw<8192>(v3, z.rs2, dw2);
      }
      if (n < 8) rdw2(8 + n, n);
      if (n == 8 || n == 9) {
        const u32 o3 = b3 - lb + (((u32)(size_t)z.q[0] - lb) & 1023) + (lane >> 5) * 1024;
        w2f[8 + n][0] = ldtr(lds + o3 + (n - 8) * 2048); w2f[8 + n][1] = ldtr(lds + (o3 ^ 64) + (n - 8) * 2048);
      }
      if (n == 13) rdw2(18, 0);
      if (n == FLN) f1 = __hip_atomic_load(flags + (rb * 2 + 1) * 16 + n16, __ATOMIC_RELAXED, __HIP_MEMORY_SCOPE_AGENT);
      if (n == 14) {
        xres[0] = *(const GAS u32x2*)(xg + ooff);
        xres[1] = *(const GAS u32x2*)(xg + ooff + 32768);
      }
      if (n >= 19 && n < 23) flush(0, n - 15);
      if (n == 34 && b == 0 && w == 0) {
#pragma unroll 1
        for (u32 spin = 0; spin < SPIN_CAP; ++spin) {
          bool ok = true;
#pragma unroll
          for (int i = 0; i < 8; ++i) ok &= __hip_atomic_load(flags + (i * 4 + g) * 32 + n16, __ATOMIC_RELAXED, __HIP_MEMORY_SCOPE_AGENT) == tag;
          if (__all(ok)) break;
          __builtin_amdgcn_s_sleep(1);
        }
        if (lane == 0) __hip_atomic_store((GAS u64*)(ws + WS_EPOCH), tag, __ATOMIC_RELAXED, __HIP_MEMORY_SCOPE_AGENT);
      }
      SB();
    }
#pragma unroll
    for (int u = 0; u < 8; ++u) flush(1, u);
    BAR();
    fetch(0);
    finish(0);
    fetch(1);
    finish(1);
  }
  asm volatile(".Lfb_end:");
}

extern "C" void kernel_launch(void* const* d_in, const int* in_sizes, int n_in, void* d_out, int out_size, void* d_ws, size_t ws_size,
                              hipStream_t stream) {
  const char *x = (const char*)d_in[0], *wqkv = (const char*)d_in[1], *wg = (const char*)d_in[4], *wu = (const char*)d_in[5];
  Args A;
  for (int i = 0; i < 8; ++i) {
    const char** a0 = A.tab[2 * i]; const char** a1 = A.tab[2 * i + 1]; const char** b0 = A.tab[16 + 2 * i]; const char** b1 = A.tab[17 + 2 * i];
    const int f0 = 1024 + 320 * i, co = f0 + (i & 1) * 64, om = (i & 1) ? f0 : f0 + 256;
    a0[0] = wqkv + i * 128;        a0[1] = wqkv + i * 128 + 1024; a0[2] = wu + i * 128; a0[3] = wu + i * 128;
    a1[0] = wqkv + i * 128 + 2048; a1[1] = wg + i * 128;          a1[2] = x;            a1[3] = x;
    b0[0] = wg + co;               b0[1] = wu + co;               b0[2] = wg + om;      b0[3] = wu + om - 64;
    b1[0] = wg + co + 128;         b1[1] = wu + co + 128;         b1[2] = x;            b1[3] = x;
    const int kb = (int)((0x7D6B105846332000ULL >> (8 * i)) & 0xff);
    A.w2[i] = kb < 32 ? (const char*)d_in[2] + kb * 16384 : (const char*)d_in[6] + (kb - 32) * 16384;
  }
  for (int j = 0; j < 16; ++j) { A.tt[0][j] = (WS5[j % 5] << 11) + 16384; A.tt[1][j] = (XS3[(j >> 1) % 3] << 11) + 8192 + (j & 1) * 4096; }
  A.tt[0][16] = 32 * 3584; A.tt[0][17] = 0; A.tt[0][18] = 32 * 3584; A.tt[0][19] = 0; A.tt[0][20] = 512 * 3584;
  A.tt[1][16] = 32768; A.tt[1][17] = 0; A.tt[1][18] = (u32)(128 - 32768); A.tt[1][19] = 0xffffffffu; A.tt[1][20] = 1024;
  for (int j = 21; j < 24; ++j) { A.tt[0][j] = 0; A.tt[1][j] = 0; }
  A.x = x; A.wo = (const char*)d_in[2]; A.wd = (const char*)d_in[6]; A.out = (char*)d_out; A.ws = (char*)d_ws;
  fused_block<<<256, 512, 0, stream>>>(A);
}
```
